```python
import torch

def _softmax(x, axis=-1):
    return torch.softmax(x, dim=axis)
def _gelu(x):
    return torch.nn.functional.gelu(x, approximate="tanh")
def _silu(x):
    return torch.nn.functional.silu(x)
def _sigmoid(x):
    return torch.sigmoid(x)
def _layernorm(x, axis=-1, eps=1e-5):
    m = x.mean(dim=axis, keepdim=True)
    v = x.var(dim=axis, keepdim=True, unbiased=False)
    return (x - m) / torch.sqrt(v + eps)
def _scatter_add(tgt, idx, src, axis=0):
    return tgt.clone().index_add_(axis, idx, src)
def _segment_sum(src, seg, n):
    out = torch.zeros((n,) + src.shape[1:], dtype=src.dtype)
    return out.index_add_(0, seg, src)
def _pad(x, pw):
    flat = tuple(v for pair in reversed(pw) for v in pair)
    return torch.nn.functional.pad(x, flat)

kernel_name = 'recurrent_scan__sliding_block_cached__dense_activation_squared_relu__decode'

def setup_inputs():

    _w = (lambda a: (lambda t: t.to(torch.bfloat16) if t.is_floating_point() else t)(torch.as_tensor(a)))
    def _A_setup():
        B, Sq, D = 2, 1, 512
        Hq, Hkv, Sk, Dh = 32, 32, 4096, 128
        assert Hq % Hkv == 0, (Hq, Hkv)
        return {
            "x": _w((lambda *s: torch.randn(*s, dtype=torch.bfloat16) * 0.05)(B, Sq, D)),
            "k_cache": _w((lambda *s: torch.randn(*s, dtype=torch.bfloat16) * 0.8)(B, Hkv, Sk, Dh)),
            "v_cache": _w((lambda *s: torch.randn(*s, dtype=torch.bfloat16) * 0.2)(B, Hkv, Sk, Dh)),
            "wq": _w((lambda *s: torch.randn(*s, dtype=torch.bfloat16) * 0.8)(D, Hq * Dh) * (D ** -0.5)),
            "wo": _w((lambda *s: torch.randn(*s, dtype=torch.bfloat16) * 0.2)(Hq * Dh, D) * ((Hq * Dh) ** -0.5)),
        }
    _A_in = _A_setup()

    def _B_setup():
        B, S, D, F = 2, 1, 512, 1376
        return {
            "x": _w((lambda *s: torch.randn(*s, dtype=torch.bfloat16) * 0.05)(B, S, D)),
            "w1": _w((lambda *s: torch.randn(*s, dtype=torch.bfloat16) * 0.1)(D, F)),
            "b1": _w((lambda *s: torch.randn(*s, dtype=torch.bfloat16) * 0.8)(F)),
            "w2": _w((lambda *s: torch.randn(*s, dtype=torch.bfloat16) * 0.1)(F, D)),
            "b2": _w((lambda *s: torch.randn(*s, dtype=torch.bfloat16) * 6.4)(D)),
        }
    _B_in = _B_setup()

    return {
        "A_x": _A_in["x"],
        "A_k_cache": _A_in["k_cache"],
        "A_v_cache": _A_in["v_cache"],
        "A_wq": _A_in["wq"],
        "A_wo": _A_in["wo"],
        "B_x": _B_in["x"],
        "B_w1": _B_in["w1"],
        "B_b1": _B_in["b1"],
        "B_w2": _B_in["w2"],
        "B_b2": _B_in["b2"],
    }

def reference(A_x, A_k_cache, A_v_cache, A_wq, A_wo, B_x, B_w1, B_b1, B_w2, B_b2):
    def _A_ref(x, k_cache, v_cache, wq, wo):
        B, Sq, D = x.shape
        Hq, Dh = 32, 128
        Hkv, Sk = k_cache.shape[1], k_cache.shape[2]
        W = min(4096, max(256, Sk // 4))
        rep = Hq // Hkv
        q = (lambda x, *d: x.permute(*d))((x @ wq).reshape(B, Sq, Hq, Dh), 0, 2, 1, 3)
        k = (lambda x, n, axis: torch.repeat_interleave(x, n, dim=axis))(k_cache[:, :, Sk - W:, :], rep, axis=1)
        v = (lambda x, n, axis: torch.repeat_interleave(x, n, dim=axis))(v_cache[:, :, Sk - W:, :], rep, axis=1)
        scores = torch.einsum("bhqd,bhkd->bhqk", q, k) * (Dh ** -0.5)
        p = _softmax(scores, axis=-1)
        o = torch.einsum("bhqk,bhkd->bhqd", p, v)
        return (lambda x, *d: x.permute(*d))(o, 0, 2, 1, 3).reshape(B, Sq, Hq * Dh) @ wo

    def _B_ref(x, w1, b1, w2, b2):
        h = torch.einsum("bsd,df->bsf", x, w1) + b1
        zero = torch.zeros_like(h)
        r = torch.where(h > zero, h, zero)
        h = r * r
        out = torch.einsum("bsf,fd->bsd", h, w2) + b2
        return out

    x = A_x
    xn = _layernorm(x, axis=-1)
    s = _A_ref(xn, A_k_cache, A_v_cache, A_wq, A_wo)
    g = _silu(_B_ref(xn, B_w1, B_b1, B_w2, B_b2))
    return x + s * g
```

```cpp
#include <hip/hip_runtime.h>
#include <hip/hip_bf16.h>
#include <stdint.h>

typedef unsigned short u16;
__device__ __forceinline__ float bf2f(u16 b){ return __uint_as_float(((unsigned)b)<<16); }
__device__ __forceinline__ u16 f2bf(float f){ __hip_bfloat16 h = __float2bfloat16(f); return *(u16*)&h; }
__device__ __forceinline__ float rbf(float f){ return bf2f(f2bf(f)); }

constexpr int D=512, H=32, SK=4096, W=1024, DH=128, F=1376, B=2;

__device__ void layernorm(const u16* x, int b, float* xn  , float* red){
  int t=threadIdx.x;
  float a=bf2f(x[b*D+t]), c=bf2f(x[b*D+t+256]);
  float s=a+c; red[t]=s; __syncthreads();
  for(int o=128;o>0;o>>=1){ if(t<o) red[t]+=red[t+o]; __syncthreads(); }
  float mean=red[0]/D; __syncthreads();
  float da=a-mean, dc=c-mean; red[t]=da*da+dc*dc; __syncthreads();
  for(int o=128;o>0;o>>=1){ if(t<o) red[t]+=red[t+o]; __syncthreads(); }
  float var=red[0]/D; __syncthreads();
  float m=rbf(mean), v=rbf(var); float sd=rbf(sqrtf(rbf(v+1e-5f)));
  xn[t]=rbf(rbf(a-m)/sd); xn[t+256]=rbf(rbf(c-m)/sd); __syncthreads();
}

__global__ void k1(const u16* x,const u16* wq,const u16* w1,const u16* b1,float* q,float* h){
  __shared__ float xn[512], red[256];
  int n = blockIdx.x*256+threadIdx.x; int b=blockIdx.y;
  layernorm(x,b,xn,red);
  if(n<4096){ float acc=0; for(int k=0;k<D;k++) acc+=xn[k]*bf2f(wq[(size_t)k*4096+n]); q[b*4096+n]=rbf(acc);}
  else { int f=n-4096; if(f<F){ float acc=0; for(int k=0;k<D;k++) acc+=xn[k]*bf2f(w1[(size_t)k*F+f]); float hh=rbf(rbf(acc)+bf2f(b1[f])); float r=hh>0?hh:0; h[b*F+f]=rbf(r*r);} }
}
__global__ void k2(const u16* kc,const u16* vc,const float* q,float* o){
  __shared__ float sc[1024], red[256], qs[128];
  int bh=blockIdx.x; int t=threadIdx.x; int b=bh/H;
  if(t<128) qs[t]=q[bh*128+t]; __syncthreads();
  const u16* K=kc+((size_t)bh*SK+(SK-W))*DH; const u16* V=vc+((size_t)bh*SK+(SK-W))*DH;
  float mx=-1e30f;
  for(int i=0;i<4;i++){ int key=t+256*i; float acc=0; for(int d=0;d<DH;d++) acc+=qs[d]*bf2f(K[(size_t)key*DH+d]); float s=rbf(rbf(acc)*0.08838834764831845f); sc[key]=s; mx=fmaxf(mx,s);}
  red[t]=mx; __syncthreads(); for(int s=128;s>0;s>>=1){ if(t<s) red[t]=fmaxf(red[t],red[t+s]); __syncthreads(); } mx=red[0]; __syncthreads();
  float sum=0; for(int i=0;i<4;i++){ int key=t+256*i; float e=__expf(sc[key]-mx); sc[key]=e; sum+=e; }
  red[t]=sum; __syncthreads(); for(int s=128;s>0;s>>=1){ if(t<s) red[t]+=red[t+s]; __syncthreads(); } sum=red[0]; __syncthreads();
  for(int i=0;i<4;i++){ int key=t+256*i; sc[key]=rbf(sc[key]/sum);} __syncthreads();
  if(t<128){ float acc=0; for(int k=0;k<W;k++) acc+=sc[k]*bf2f(V[(size_t)k*DH+t]); o[bh*128+t]=rbf(acc);}
}
__global__ void k3(const u16* x,const u16* wo,const u16* w2,const u16* b2,const float* o,const float* h,u16* out){
  int b=blockIdx.y; int c=blockIdx.x*64+threadIdx.x; float s=0,g=0;
  for(int k=0;k<4096;k++) s+=o[b*4096+k]*bf2f(wo[(size_t)k*D+c]);
  for(int k=0;k<F;k++) g+=h[b*F+k]*bf2f(w2[(size_t)k*D+c]);
  s=rbf(s); g=rbf(rbf(g)+bf2f(b2[c])); float sg=rbf(g/(1.f+__expf(-g)));
  out[b*D+c]=f2bf(bf2f(x[b*D+c])+rbf(s*sg));
}
extern "C" void kernel_launch(void* const* d_in, const int* in_sizes, int n_in, void* d_out, int out_size, void* d_ws, size_t ws_size, hipStream_t stream){
  const u16* x=(const u16*)d_in[0]; const u16* kc=(const u16*)d_in[1]; const u16* vc=(const u16*)d_in[2]; const u16* wq=(const u16*)d_in[3]; const u16* wo=(const u16*)d_in[4];
  const u16* w1=(const u16*)d_in[6]; const u16* b1=(const u16*)d_in[7]; const u16* w2=(const u16*)d_in[8]; const u16* b2=(const u16*)d_in[9];
  float* ws=(float*)d_ws; float* q=ws; float* h=ws+8192; float* o=ws+16384;
  k1<<<dim3(22,2),256,0,stream>>>(x,wq,w1,b1,q,h);
  k2<<<64,256,0,stream>>>(kc,vc,q,o);
  k3<<<dim3(8,2),64,0,stream>>>(x,wo,w2,b2,o,h,(u16*)d_out);
}
```

```cpp
#include <hip/hip_runtime.h>
#include <stdint.h>

typedef unsigned u32;
typedef unsigned long long u64;
typedef unsigned short u16;
typedef u32 v4u __attribute__((ext_vector_type(4)));
typedef u32 v2u __attribute__((ext_vector_type(2)));
typedef float v4f __attribute__((ext_vector_type(4)));
typedef float v2f __attribute__((ext_vector_type(2)));
typedef short s8v __attribute__((ext_vector_type(8)));
typedef short s4v __attribute__((ext_vector_type(4)));
typedef __bf16 bf2v __attribute__((ext_vector_type(2)));
#define LAS __attribute__((address_space(3)))
#define CAS __attribute__((address_space(4)))

#define KV_NT " nt"
#define W_NT " nt"
#define DLY1 6
#define DLY2 6
#define SPIN_CAP 200000

constexpr u32 WS_QP = 4096;
constexpr u32 WS_HP = WS_QP + 32 * 8 * 2 * 1024;
constexpr u32 WS_OP = WS_HP + 43 * 4 * 512 + 2048;
constexpr u32 WS_SP = WS_OP + 256 * 1152 + 4096;
static_assert(WS_HP % 128 == 0 && WS_OP % 128 == 0 && WS_SP % 128 == 0, "line aligned");
constexpr u32 L_WQ = 0;
constexpr u32 L_W1 = 16384;
constexpr u32 L_WO = 24576;
constexpr u32 L_W2 = 40960;
constexpr u32 L_XN = 49152;
constexpr u32 L_QB = 51200;
constexpr u32 L_HB = 51456;
constexpr u32 L_ON = 51712;
constexpr u32 L_MX = 52224;
constexpr u32 L_SM = 52256;
constexpr u32 L_RED = 52352;
constexpr u32 L_BYTES = 86016;

__device__ __forceinline__ v4f mfma16(v4u a, v4u b, v4f c) {
  return __builtin_amdgcn_mfma_f32_16x16x32_bf16(__builtin_bit_cast(s8v, a), __builtin_bit_cast(s8v, b), c, 0, 0, 0);
}
__device__ __forceinline__ u32 pk2(float lo, float hi) { bf2v z = {(__bf16)lo, (__bf16)hi}; return __builtin_bit_cast(u32, z); }
__device__ __forceinline__ float rbf(float x) { return __uint_as_float(pk2(x, x) & 0xffff0000u); }
__device__ __forceinline__ float bflo(u32 v) { return __uint_as_float(v << 16); }
__device__ __forceinline__ float bfhi(u32 v) { return __uint_as_float(v & 0xffff0000u); }
template <int CTRL> __device__ __forceinline__ float dppf(float x) {
  return __builtin_bit_cast(float, __builtin_amdgcn_mov_dpp(__builtin_bit_cast(int, x), CTRL, 0xf, 0xf, true));
}
template <int CTRL> __device__ __forceinline__ u32 dppu(u32 x) { return (u32)__builtin_amdgcn_mov_dpp((int)x, CTRL, 0xf, 0xf, true); }
constexpr int XOR1 = 0xB1, XOR2 = 0x4E, HMIR = 0x141, MIR = 0x140, ROR4 = 0x124, ROR8 = 0x128, BC0 = 0x150;
__device__ __forceinline__ float x16sum(float x) {
  auto s = __builtin_amdgcn_permlane16_swap(__float_as_uint(x), __float_as_uint(x), false, false);
  x = __uint_as_float(s[0]) + __uint_as_float(s[1]);
  auto t = __builtin_amdgcn_permlane32_swap(__float_as_uint(x), __float_as_uint(x), false, false);
  return __uint_as_float(t[0]) + __uint_as_float(t[1]);
}
__device__ __forceinline__ float x16max(float x) {
  auto s = __builtin_amdgcn_permlane16_swap(__float_as_uint(x), __float_as_uint(x), false, false);
  x = fmaxf(__uint_as_float(s[0]), __uint_as_float(s[1]));
  auto t = __builtin_amdgcn_permlane32_swap(__float_as_uint(x), __float_as_uint(x), false, false);
  return fmaxf(__uint_as_float(t[0]), __uint_as_float(t[1]));
}
__device__ __forceinline__ float wave_sum(float x) {
  x += dppf<XOR1>(x); x += dppf<XOR2>(x); x += dppf<HMIR>(x); x += dppf<MIR>(x);
  return x16sum(x);
}
#define BARRIER() asm volatile("s_waitcnt lgkmcnt(0)\n\ts_barrier" ::: "memory")
#define VMWAIT(n) asm volatile("s_waitcnt vmcnt(" #n ")" ::: "memory")

#define DMA16(voff, base, lds, POL) \
  asm volatile("s_mov_b32 m0, %2\n\ts_nop 0\n\tglobal_load_lds_dwordx4 %0, %1" POL :: "v"(voff), "s"(base), "s"(lds) : "memory")
#define LD16(dst, voff, base, imm, POL, NAME) \
  asm volatile("global_load_dwordx4 %0, %1, %2 offset:" #imm POL " ; LD " NAME : "=v"(dst) : "v"(voff), "s"(base) : "memory")

__device__ __forceinline__ v4u lds128(const LAS unsigned char* L, u32 off) { return *(const LAS v4u*)(L + off); }
__device__ __forceinline__ v4u trfrag(const LAS unsigned char* L, u32 off, u32 step4) {
  s4v lo = __builtin_amdgcn_ds_read_tr16_b64_v4i16((LAS s4v*)(L + off));
  s4v hi = __builtin_amdgcn_ds_read_tr16_b64_v4i16((LAS s4v*)(L + off + step4));
  v2u a = __builtin_bit_cast(v2u, lo), b = __builtin_bit_cast(v2u, hi);
  return v4u{a.x, a.y, b.x, b.y};
}

#define STAMP(i) do {} while (0)

__global__ void __launch_bounds__(512, 2)
fused_decode(const u16* __restrict__ x, const u16* __restrict__ kc, const u16* __restrict__ vc, const u16* __restrict__ wq,
             const u16* __restrict__ wo, const u16* __restrict__ w1, const u16* __restrict__ b1, const u16* __restrict__ w2,
             const u16* __restrict__ b2, u16* __restrict__ out, unsigned char* __restrict__ ws) {
  __shared__ __attribute__((aligned(16))) unsigned char smem[L_BYTES];
  LAS unsigned char* L = (LAS unsigned char*)smem;
  const u32 ldsb = (u32)(size_t)smem;
  STAMP(0);
  asm volatile("" :: "s"(x), "s"(kc), "s"(vc), "s"(wq), "s"(wo), "s"(w1), "s"(b1), "s"(w2), "s"(b2), "s"(out), "s"(ws));
  const u32 tid = threadIdx.x, lane = tid & 63;
  const u32 wv = __builtin_amdgcn_readfirstlane(tid >> 6);
  const u32 w = blockIdx.x, xcd = w & 7, r = w >> 3;
  const u32 h = 4 * xcd + (r >> 3), j = r & 7, b = j >> 2, sp = j & 3;
  const u32 g = lane >> 4, i16 = lane & 15;
  const u32 cmb = (r >> 1) * 8 + xcd, kap = 2 * (cmb >> 2) + (r & 1), kq = cmb & 3;
  const bool ffn = (r < 22) && (kap < 43);
  const u32 nk = r < 21 ? 1 : 2, kap2 = r < 21 ? r : 21 + 2 * (r - 21);
  const u32 tag = *(const CAS u32*)ws + 1u;

  v4u xr;
  if (wv < 2) { const u32 vo = wv * 1024 + lane * 16; LD16(xr, vo, x, 0, "", "X"); }
  {
    const u32 vo = (64 * j + 8 * wv + g) * 8192 + h * 256 + i16 * 16;
    DMA16(vo, wq, ldsb + L_WQ + wv * 2048, W_NT);
    const u32 vo2 = vo + 4 * 8192;
    DMA16(vo2, wq, ldsb + L_WQ + wv * 2048 + 1024, W_NT);
  }
  if (ffn) {
    const u32 vo = (128 * kq + 16 * wv + (lane >> 2)) * 2752 + kap * 64 + (lane & 3) * 16;
    DMA16(vo, w1, ldsb + L_W1 + wv * 1024, W_NT);
  }
  const u32 kvbase = ((b * 32 + h) * 4096 + 3072 + sp * 256 + wv * 32) * 256 + 4096;
  const u32 vK = kvbase + ((lane >> 2) & 3) * 256 + (4 * g + (lane & 3)) * 16;
  const u32 vV = kvbase + g * 1024 + i16 * 16;
  v4u K[8], V[8];
  LD16(K[0], vK, kc, -4096, KV_NT, "K0");
  LD16(K[1], vK, kc, -3072, KV_NT, "K1");
  LD16(K[2], vK, kc, -2048, KV_NT, "K2");
  LD16(K[3], vK, kc, -1024, KV_NT, "K3");
  STAMP(1);
  if (wv < 2) {
    asm volatile("s_waitcnt vmcnt(6) ; GUARD X" : "+v"(xr) :: "memory");
    float v[8] = {bflo(xr.x), bfhi(xr.x), bflo(xr.y), bfhi(xr.y), bflo(xr.z), bfhi(xr.z), bflo(xr.w), bfhi(xr.w)};
    float s = ((v[0] + v[1]) + (v[2] + v[3])) + ((v[4] + v[5]) + (v[6] + v[7]));
    const float mean = wave_sum(s) * (1.f / 512.f);
    float q = 0.f;
#pragma unroll
    for (int k = 0; k < 8; k++) { const float d = v[k] - mean; q = fmaf(d, d, q); }
    const float var = wave_sum(q) * (1.f / 512.f);
    const float m = rbf(mean);
    const float sd = rbf(__builtin_amdgcn_sqrtf(rbf(rbf(var) + 1e-5f)));
    const float inv = __builtin_amdgcn_rcpf(sd);
    v4u o;
    o.x = pk2(rbf(v[0] - m) * inv, rbf(v[1] - m) * inv);
    o.y = pk2(rbf(v[2] - m) * inv, rbf(v[3] - m) * inv);
    o.z = pk2(rbf(v[4] - m) * inv, rbf(v[5] - m) * inv);
    o.w = pk2(rbf(v[6] - m) * inv, rbf(v[7] - m) * inv);
    *(LAS v4u*)(L + L_XN + wv * 1024 + lane * 16) = o;
  }
  VMWAIT(4);
  BARRIER();
  STAMP(2);
  const u32 trq = (i16 >> 2), trp = lane & 3;
  {
    const u32 aoff = L_XN + (lane & 1) * 1024 + (64 * j + 8 * g) * 2;
    const u32 boff = L_WQ + (8 * g + trq) * 256 + wv * 32 + trp * 8;
    v4f d = {0.f, 0.f, 0.f, 0.f};
    d = mfma16(lds128(L, aoff), trfrag(L, boff, 1024), d);
    d = mfma16(lds128(L, aoff + 64), trfrag(L, boff + 32 * 256, 1024), d);
    if (lane < 16) {
      const u32 vo = WS_QP + (h * 8 + j) * 2048 + (16 * wv + lane) * 8;
      const v2u g0 = {__float_as_uint(d[0]), tag}, g1 = {__float_as_uint(d[1]), tag};
      asm volatile("global_store_dwordx2 %0, %1, %3 sc1\n\tglobal_store_dwordx2 %0, %2, %3 offset:1024 sc1" :: "v"(vo), "v"(g0), "v"(g1), "s"(ws) : "memory");
    }
  }
  if (ffn && (wv == 2 || wv == 3)) {
    const u32 c = wv - 2;
    const u32 aoff = L_XN + (lane & 1) * 1024 + (128 * kq + 8 * g) * 2;
    const u32 boff = L_W1 + (8 * g + trq) * 64 + c * 32 + trp * 8;
    v4f d = {0.f, 0.f, 0.f, 0.f};
#pragma unroll
    for (int ks = 0; ks < 4; ks++) d = mfma16(lds128(L, aoff + ks * 64), trfrag(L, boff + ks * 32 * 64, 256), d);
    if (lane < 16) {
      const u32 vo = WS_HP + (kap * 4 + kq) * 512 + (16 * c + lane) * 8;
      const v2u g0 = {__float_as_uint(d[0]), tag}, g1 = {__float_as_uint(d[1]), tag};
      asm volatile("global_store_dwordx2 %0, %1, %3 sc1\n\tglobal_store_dwordx2 %0, %2, %3 offset:256 sc1" :: "v"(vo), "v"(g0), "v"(g1), "s"(ws) : "memory");
    }
  }
  LD16(K[4], vK, kc, 0, KV_NT, "K4");
  LD16(K[5], vK, kc, 1024, KV_NT, "K5");
  LD16(K[6], vK, kc, 2048, KV_NT, "K6");
  LD16(K[7], vK, kc, 3072, KV_NT, "K7");
  LD16(V[0], vV, vc, -4096, KV_NT, "V0");
  LD16(V[1], vV, vc, -3840, KV_NT, "V1");
  LD16(V[2], vV, vc, -3584, KV_NT, "V2");
  LD16(V[3], vV, vc, -3328, KV_NT, "V3");
  LD16(V[4], vV, vc, 0, KV_NT, "V4");
  LD16(V[5], vV, vc, 256, KV_NT, "V5");
  LD16(V[6], vV, vc, 512, KV_NT, "V6");
  LD16(V[7], vV, vc, 768, KV_NT, "V7");
  STAMP(3);
  if (wv == 0) {
    VMWAIT(8);
    const u32 vo = WS_QP + h * 8 * 2048 + b * 1024 + lane * 16 + 4096, vo2 = vo + 8192;
    float q0, q1;
    for (u32 spin = 0;; spin++) {
      v4u p0, p1, p2, p3, p4, p5, p6, p7;
      asm volatile(
          "global_load_dwordx4 %0, %8, %10 offset:-4096 sc1\n\t"
          "global_load_dwordx4 %1, %8, %10 offset:-2048 sc1\n\t"
          "global_load_dwordx4 %2, %8, %10 sc1\n\t"
          "global_load_dwordx4 %3, %8, %10 offset:2048 sc1\n\t"
          "global_load_dwordx4 %4, %9, %10 offset:-4096 sc1\n\t"
          "global_load_dwordx4 %5, %9, %10 offset:-2048 sc1\n\t"
          "global_load_dwordx4 %6, %9, %10 sc1\n\t"
          "global_load_dwordx4 %7, %9, %10 offset:2048 sc1\n\t"
          "s_waitcnt vmcnt(0)"
          : "=&v"(p0), "=&v"(p1), "=&v"(p2), "=&v"(p3), "=&v"(p4), "=&v"(p5), "=&v"(p6), "=&v"(p7)
          : "v"(vo), "v"(vo2), "s"(ws) : "memory");
      const u32 bad = ((p0.y ^ tag) | (p0.w ^ tag) | (p1.y ^ tag) | (p1.w ^ tag)) | ((p2.y ^ tag) | (p2.w ^ tag) | (p3.y ^ tag) | (p3.w ^ tag)) |
                      ((p4.y ^ tag) | (p4.w ^ tag) | (p5.y ^ tag) | (p5.w ^ tag)) | ((p6.y ^ tag) | (p6.w ^ tag) | (p7.y ^ tag) | (p7.w ^ tag));
      q0 = ((__uint_as_float(p0.x) + __uint_as_float(p1.x)) + (__uint_as_float(p2.x) + __uint_as_float(p3.x))) +
           ((__uint_as_float(p4.x) + __uint_as_float(p5.x)) + (__uint_as_float(p6.x) + __uint_as_float(p7.x)));
      q1 = ((__uint_as_float(p0.z) + __uint_as_float(p1.z)) + (__uint_as_float(p2.z) + __uint_as_float(p3.z))) +
           ((__uint_as_float(p4.z) + __uint_as_float(p5.z)) + (__uint_as_float(p6.z) + __uint_as_float(p7.z)));
      if (__all(bad == 0) || spin > SPIN_CAP) break;
      __builtin_amdgcn_s_sleep(2);
    }
    *(LAS u32*)(L + L_QB + lane * 4) = pk2(q0, q1);
    STAMP(4);
  } else if (wv == 1) {
    VMWAIT(8);
    const u32 bb = lane >> 5, f = lane & 31;
    for (u32 ki = 0; ki < nk; ki++) {
      const u32 kp = kap2 + ki;
      const u32 vo = WS_HP + kp * 2048 + bb * 256 + f * 8;
      const u32 vb = (kp * 32 + f) * 2;
      float hs; u32 bias;
      for (u32 spin = 0;; spin++) {
        v2u p0, p1, p2, p3;
        asm volatile(
            "global_load_ushort %4, %6, %8\n\t"
            "global_load_dwordx2 %0, %5, %7 sc1\n\t"
            "global_load_dwordx2 %1, %5, %7 offset:512 sc1\n\t"
            "global_load_dwordx2 %2, %5, %7 offset:1024 sc1\n\t"
            "global_load_dwordx2 %3, %5, %7 offset:1536 sc1\n\t"
            "s_waitcnt vmcnt(0)"
            : "=&v"(p0), "=&v"(p1), "=&v"(p2), "=&v"(p3), "=&v"(bias)
            : "v"(vo), "v"(vb), "s"(ws), "s"(b1) : "memory");
        const u32 bad = (p0.y ^ tag) | (p1.y ^ tag) | (p2.y ^ tag) | (p3.y ^ tag);
        hs = (__uint_as_float(p0.x) + __uint_as_float(p1.x)) + (__uint_as_float(p2.x) + __uint_as_float(p3.x));
        if (__all(bad == 0) || spin > SPIN_CAP) break;
        __builtin_amdgcn_s_sleep(2);
      }
      float hv = rbf(rbf(hs) + bflo(bias));
      hv = hv > 0.f ? hv : 0.f;
      *(LAS u16*)(L + L_HB + bb * 128 + ki * 64 + f * 2) = (u16)pk2(hv * hv, 0.f);
    }
  }
  BARRIER();
  STAMP(5);
  float t0, t1;
  {
    const v4u qc = lds128(L, L_QB + (4 * g + (lane & 3)) * 16);
    v4u A[4];
#pragma unroll
    for (int a = 0; a < 4; a++) {
      const bool on = (i16 >> 2) == (u32)a;
      A[a] = v4u{on ? qc.x : 0u, on ? qc.y : 0u, on ? qc.z : 0u, on ? qc.w : 0u};
    }
    asm volatile("s_waitcnt vmcnt(8) ; GUARD K0 K1 K2 K3 K4 K5 K6 K7" : "+v"(K[0]), "+v"(K[1]), "+v"(K[2]), "+v"(K[3]), "+v"(K[4]), "+v"(K[5]), "+v"(K[6]), "+v"(K[7]) :: "memory");
    v4f d0 = {0.f, 0.f, 0.f, 0.f}, d1 = {0.f, 0.f, 0.f, 0.f};
#pragma unroll
    for (int a = 0; a < 4; a++) { d0 = mfma16(A[a], K[a], d0); d1 = mfma16(A[a], K[4 + a], d1); }
    const bool s1 = lane & 1, s2 = lane & 2;
    float u0 = s2 ? (s1 ? d0[3] : d0[2]) : (s1 ? d0[1] : d0[0]);
    float u1 = s2 ? (s1 ? d1[3] : d1[2]) : (s1 ? d1[1] : d1[0]);
    u0 += dppf<XOR1>(u0); u1 += dppf<XOR1>(u1);
    u0 += dppf<XOR2>(u0); u1 += dppf<XOR2>(u1);
    constexpr float SC = 0.08838834764831845f, L2E = 1.4426950408889634f;
    t0 = rbf(rbf(u0) * SC) * L2E;
    t1 = rbf(rbf(u1) * SC) * L2E;
  }
  {
    float m = fmaxf(t0, t1);
    m = fmaxf(m, dppf<ROR4>(m)); m = fmaxf(m, dppf<ROR8>(m));
    m = x16max(m);
    if (lane == 0) *(LAS float*)(L + L_MX + wv * 4) = m;
  }
  BARRIER();
  STAMP(6);
  {
    const u32 vo = (r * 128 + 16 * wv + (lane >> 3)) * 1024 + xcd * 128 + (lane & 7) * 16;
    DMA16(vo, wo, ldsb + L_WO + wv * 2048, W_NT);
    const u32 vo2 = vo + 8 * 1024;
    DMA16(vo2, wo, ldsb + L_WO + wv * 2048 + 1024, W_NT);
    const u32 lw2 = __builtin_amdgcn_readfirstlane(ldsb + L_W2 + wv * 512 * nk);
    const u32 vw = (kap2 * 32 + wv * 4 * nk + (lane >> 3)) * 1024 + xcd * 128 + (lane & 7) * 16;
    asm volatile("" :: "s"(lw2));
    if (lane < 32 * nk) {
      DMA16(vw, w2, lw2, W_NT);
    }
  }
  float M;
  v4u AV[8];
  {
    const v4f ma = *(const LAS v4f*)(L + L_MX), mb = *(const LAS v4f*)(L + L_MX + 16);
    M = fmaxf(fmaxf(fmaxf(ma[0], ma[1]), fmaxf(ma[2], ma[3])), fmaxf(fmaxf(mb[0], mb[1]), fmaxf(mb[2], mb[3])));
    const float p0 = __builtin_amdgcn_exp2f(t0 - M), p1 = __builtin_amdgcn_exp2f(t1 - M);
    float ls = p0 + p1;
    ls += dppf<ROR4>(ls); ls += dppf<ROR8>(ls);
    ls = x16sum(ls);
    if (lane == 0) *(LAS float*)(L + L_SM + wv * 4) = ls;
    const u32 pp0 = pk2(p0, p0), pp1 = pk2(p1, p1);
    const u32 hm = (lane & 1) ? 0xffff0000u : 0x0000ffffu, kd = (lane & 7) >> 1;
    const u32 mk0 = kd == 0 ? hm : 0u, mk1 = kd == 1 ? hm : 0u, mk2 = kd == 2 ? hm : 0u, mk3 = kd == 3 ? hm : 0u;
#define AVB(t, pp, c) { const u32 z = dppu<BC0 + c>(pp); AV[t] = v4u{z & mk0, z & mk1, z & mk2, z & mk3}; }
    AVB(0, pp0, 0) AVB(1, pp0, 4) AVB(2, pp0, 8) AVB(3, pp0, 12) AVB(4, pp1, 0) AVB(5, pp1, 4) AVB(6, pp1, 8) AVB(7, pp1, 12)
#undef AVB
    asm volatile("" : "+v"(AV[0]), "+v"(AV[1]), "+v"(AV[2]), "+v"(AV[3]), "+v"(AV[4]), "+v"(AV[5]), "+v"(AV[6]), "+v"(AV[7]));
  }
  {
    v4f d = {0.f, 0.f, 0.f, 0.f};
#define PVS(t, n) asm volatile("s_waitcnt vmcnt(" #n ") ; GUARD V" #t : "+v"(V[t]) :: "memory"); d = mfma16(AV[t], V[t], d); __builtin_amdgcn_sched_barrier(0);
    PVS(0, 10) PVS(1, 9) PVS(2, 8) PVS(3, 7) PVS(4, 6) PVS(5, 5) PVS(6, 4) PVS(7, 3)
#undef PVS
    if (lane < 32) *(LAS v4f*)(L + L_RED + wv * 512 + (8 * i16 + 4 * g) * 4) = d;
  }
  BARRIER();
  STAMP(7);
  v4u P0, P1, P2, P3, P4, P5, P6, P7, PM;
  const u32 voP = WS_OP + r * 8 * 1152 + lane * 16 + 4032, voM = WS_OP + (r * 8 + (lane & 7)) * 1152 + 1024;
  if (wv == 0) {
    float o0 = 0.f, o1 = 0.f;
#pragma unroll
    for (int k = 0; k < 8; k++) { const v2f v = *(const LAS v2f*)(L + L_RED + k * 512 + lane * 8); o0 += v.x; o1 += v.y; }
    const u32 vo = WS_OP + (h * 8 + j) * 1152 + lane * 16;
    const v4u gq = {__float_as_uint(o0), tag, __float_as_uint(o1), tag};
    asm volatile("global_store_dwordx4 %0, %1, %2 sc1\n\ts_nop 1" :: "v"(vo), "v"(gq), "s"(ws) : "memory");
    VMWAIT(1);
  } else if (wv == 1) {
    const v4f sa = *(const LAS v4f*)(L + L_SM), sb = *(const LAS v4f*)(L + L_SM + 16);
    const float ls = (((sa[0] + sa[1]) + (sa[2] + sa[3])) + ((sb[0] + sb[1]) + (sb[2] + sb[3])));
    if (lane == 0) {
      const u32 vo = WS_OP + (h * 8 + j) * 1152 + 1024;
      const v4u gq = {__float_as_uint(M), tag, __float_as_uint(ls), tag};
      asm volatile("global_store_dwordx4 %0, %1, %2 sc1\n\ts_nop 1" :: "v"(vo), "v"(gq), "s"(ws) : "memory");
    }
    VMWAIT(1);
  } else if (wv == 2) {
    __builtin_amdgcn_s_sleep(DLY1);
    asm volatile(
        "global_load_dwordx4 %8, %10, %11 sc1 ; LD PM\n\t"
        "global_load_dwordx4 %0, %9, %11 offset:-4032 sc1 ; LD P0\n\t"
        "global_load_dwordx4 %1, %9, %11 offset:-2880 sc1 ; LD P1\n\t"
        "global_load_dwordx4 %2, %9, %11 offset:-1728 sc1 ; LD P2\n\t"
        "global_load_dwordx4 %3, %9, %11 offset:-576 sc1 ; LD P3\n\t"
        "global_load_dwordx4 %4, %9, %11 offset:576 sc1 ; LD P4\n\t"
        "global_load_dwordx4 %5, %9, %11 offset:1728 sc1 ; LD P5\n\t"
        "global_load_dwordx4 %6, %9, %11 offset:2880 sc1 ; LD P6\n\t"
        "global_load_dwordx4 %7, %9, %11 offset:4032 sc1 ; LD P7\n\t"
        "s_waitcnt vmcnt(9)"
        : "=&v"(P0), "=&v"(P1), "=&v"(P2), "=&v"(P3), "=&v"(P4), "=&v"(P5), "=&v"(P6), "=&v"(P7), "=&v"(PM)
        : "v"(voP), "v"(voM), "s"(ws) : "memory");
  } else {
    VMWAIT(0);
  }
  BARRIER();
  STAMP(8);
  v4u BW[4];
  v4f dg = {0.f, 0.f, 0.f, 0.f};
  if (wv >= 4) {
    const u32 t = wv - 4;
    const u32 boff = (8 * g + trq) * 128 + t * 32 + trp * 8;
#pragma unroll
    for (int ks = 0; ks < 4; ks++) BW[ks] = trfrag(L, L_WO + boff + ks * 4096, 512);
    for (u32 ki = 0; ki < nk; ki++)
      dg = mfma16(lds128(L, L_HB + (lane & 1) * 128 + ki * 64 + g * 16), trfrag(L, L_W2 + boff + ki * 4096, 512), dg);
  } else if (wv == 2) {
    asm volatile("s_waitcnt vmcnt(0) ; GUARD P0 P1 P2 P3 P4 P5 P6 P7 PM" : "+v"(P0), "+v"(P1), "+v"(P2), "+v"(P3), "+v"(P4), "+v"(P5), "+v"(P6), "+v"(P7), "+v"(PM) :: "memory");
    v4u p0 = P0, p1 = P1, p2 = P2, p3 = P3, p4 = P4, p5 = P5, p6 = P6, p7 = P7, pm = PM;
    for (u32 spin = 0;; spin++) {
      const u32 bad = ((p0.y ^ tag) | (p0.w ^ tag) | (p1.y ^ tag) | (p1.w ^ tag)) | ((p2.y ^ tag) | (p2.w ^ tag) | (p3.y ^ tag) | (p3.w ^ tag)) |
                      ((p4.y ^ tag) | (p4.w ^ tag) | (p5.y ^ tag) | (p5.w ^ tag)) | ((p6.y ^ tag) | (p6.w ^ tag) | (p7.y ^ tag) | (p7.w ^ tag)) |
                      ((pm.y ^ tag) | (pm.w ^ tag));
      if (__all(bad == 0) || spin > SPIN_CAP) break;
      asm volatile(
          "global_load_dwordx4 %8, %10, %11 sc1\n\t"
          "global_load_dwordx4 %0, %9, %11 offset:-4032 sc1\n\t"
          "global_load_dwordx4 %1, %9, %11 offset:-2880 sc1\n\t"
          "global_load_dwordx4 %2, %9, %11 offset:-1728 sc1\n\t"
          "global_load_dwordx4 %3, %9, %11 offset:-576 sc1\n\t"
          "global_load_dwordx4 %4, %9, %11 offset:576 sc1\n\t"
          "global_load_dwordx4 %5, %9, %11 offset:1728 sc1\n\t"
          "global_load_dwordx4 %6, %9, %11 offset:2880 sc1\n\t"
          "global_load_dwordx4 %7, %9, %11 offset:4032 sc1\n\t"
          "s_waitcnt vmcnt(0)"
          : "=&v"(p0), "=&v"(p1), "=&v"(p2), "=&v"(p3), "=&v"(p4), "=&v"(p5), "=&v"(p6), "=&v"(p7), "=&v"(pm)
          : "v"(voP), "v"(voM), "s"(ws) : "memory");
    }
    STAMP(9);
    const float mp = __uint_as_float(pm.x), lp = __uint_as_float(pm.z);
    float Mb = fmaxf(mp, dppf<XOR1>(mp)); Mb = fmaxf(Mb, dppf<XOR2>(Mb));
    const float e = __builtin_amdgcn_exp2f(mp - Mb);
    float Lb = e * lp; Lb += dppf<XOR1>(Lb); Lb += dppf<XOR2>(Lb);
    const float f = e * __builtin_amdgcn_rcpf(Lb);
    const float f0 = dppf<BC0 + 0>(f), f1 = dppf<BC0 + 1>(f), f2 = dppf<BC0 + 2>(f), f3 = dppf<BC0 + 3>(f);
    const float f4 = dppf<BC0 + 4>(f), f5 = dppf<BC0 + 5>(f), f6 = dppf<BC0 + 6>(f), f7 = dppf<BC0 + 7>(f);
    const float a0 = fmaf(f0, __uint_as_float(p0.x), f1 * __uint_as_float(p1.x)) + fmaf(f2, __uint_as_float(p2.x), f3 * __uint_as_float(p3.x));
    const float a1 = fmaf(f0, __uint_as_float(p0.z), f1 * __uint_as_float(p1.z)) + fmaf(f2, __uint_as_float(p2.z), f3 * __uint_as_float(p3.z));
    const float c0 = fmaf(f4, __uint_as_float(p4.x), f5 * __uint_as_float(p5.x)) + fmaf(f6, __uint_as_float(p6.x), f7 * __uint_as_float(p7.x));
    const float c1 = fmaf(f4, __uint_as_float(p4.z), f5 * __uint_as_float(p5.z)) + fmaf(f6, __uint_as_float(p6.z), f7 * __uint_as_float(p7.z));
    *(LAS u32*)(L + L_ON + lane * 4) = pk2(a0, a1);
    *(LAS u32*)(L + L_ON + 256 + lane * 4) = pk2(c0, c1);
  }
  BARRIER();
  STAMP(10);
  if (wv >= 4) {
    const u32 t = wv - 4;
    const u32 aoff = L_ON + (lane & 1) * 256 + g * 16;
    v4f d = {0.f, 0.f, 0.f, 0.f};
#pragma unroll
    for (int ks = 0; ks < 4; ks++) d = mfma16(lds128(L, aoff + ks * 64), BW[ks], d);
    auto ss = __builtin_amdgcn_permlane16_swap(__float_as_uint(d[0]), __float_as_uint(d[1]), false, false);
    auto gg = __builtin_amdgcn_permlane16_swap(__float_as_uint(dg[0]), __float_as_uint(dg[1]), false, false);
    if (lane < 32) {
      const u32 vo = WS_SP + ((xcd * 32 + r) * 128 + g * 64 + 16 * t + i16) * 16;
      const v4u gq = {ss[0], tag, gg[0], tag};
      asm volatile("global_store_dwordx4 %0, %1, %2 sc1\n\ts_nop 1" :: "v"(vo), "v"(gq), "s"(ws) : "memory");
    }
    STAMP(11);
  } else if (wv == 0 && r < 16) {
    const u32 bb = r >> 3, col = xcd * 64 + (r & 7) * 8 + (lane & 7);
    const u32 vo = WS_SP + ((xcd * 32 + (lane >> 3)) * 128 + bb * 64 + (r & 7) * 8 + (lane & 7)) * 16;
    const u32 vo1 = vo + 8 * 2048, vo2 = vo + 16 * 2048, vo3 = vo + 24 * 2048;
    const u32 vx = (bb * 512 + col) * 2, vb = col * 2;
    u32 xv, bv;
    asm volatile("global_load_ushort %0, %2, %4 ; LD XV\n\tglobal_load_ushort %1, %3, %5 ; LD BV" : "=&v"(xv), "=&v"(bv) : "v"(vx), "v"(vb), "s"(x), "s"(b2) : "memory");
    __builtin_amdgcn_s_sleep(DLY2);
    float s, gs;
    for (u32 spin = 0;; spin++) {
      v4u p0, p1, p2, p3;
      asm volatile(
          "global_load_dwordx4 %0, %4, %8 sc1\n\t"
          "global_load_dwordx4 %1, %5, %8 sc1\n\t"
          "global_load_dwordx4 %2, %6, %8 sc1\n\t"
          "global_load_dwordx4 %3, %7, %8 sc1\n\t"
          "s_waitcnt vmcnt(0)"
          : "=&v"(p0), "=&v"(p1), "=&v"(p2), "=&v"(p3)
          : "v"(vo), "v"(vo1), "v"(vo2), "v"(vo3), "s"(ws) : "memory");
      const u32 bad = ((p0.y ^ tag) | (p0.w ^ tag) | (p1.y ^ tag) | (p1.w ^ tag)) | ((p2.y ^ tag) | (p2.w ^ tag) | (p3.y ^ tag) | (p3.w ^ tag));
      s = (__uint_as_float(p0.x) + __uint_as_float(p1.x)) + (__uint_as_float(p2.x) + __uint_as_float(p3.x));
      gs = (__uint_as_float(p0.z) + __uint_as_float(p1.z)) + (__uint_as_float(p2.z) + __uint_as_float(p3.z));
      if (__all(bad == 0) || spin > SPIN_CAP) break;
    }
    asm volatile("; GUARD XV BV" : "+v"(xv), "+v"(bv));
    s += dppf<ROR8>(s); gs += dppf<ROR8>(gs);
    s = x16sum(s); gs = x16sum(gs);
    const float gt = rbf(rbf(gs) + bflo(bv));
    const float sg = rbf(gt * __builtin_amdgcn_rcpf(1.f + __builtin_amdgcn_exp2f(gt * -1.4426950408889634f)));
    const float o = bflo(xv) + rbf(rbf(s) * sg);
    if (lane < 8) {
      const u32 vo_ = (bb * 512 + col) * 2;
      const u32 ob = pk2(o, o);
      asm volatile("global_store_short %0, %1, %2" :: "v"(vo_), "v"(ob), "s"(out) : "memory");
    }
    if (w == 0 && lane == 0) {
      const u32 z = 0;
      asm volatile("global_store_dword %0, %1, %2 sc1" :: "v"(z), "v"(tag), "s"(ws) : "memory");
    }
    STAMP(12);
  }
}

extern "C" void kernel_launch(void* const* d_in, const int* in_sizes, int n_in, void* d_out, int out_size, void* d_ws, size_t ws_size,
                              hipStream_t stream) {
  fused_decode<<<256, 512, 0, stream>>>((const u16*)d_in[0], (const u16*)d_in[1], (const u16*)d_in[2], (const u16*)d_in[3], (const u16*)d_in[4],
                                        (const u16*)d_in[6], (const u16*)d_in[7], (const u16*)d_in[8], (const u16*)d_in[9], (u16*)d_out,
                                        (unsigned char*)d_ws);
}
```

```cpp
#define PROBE_TH 2100
#include <hip/hip_runtime.h>
#include <stdint.h>

typedef unsigned u32;
typedef unsigned long long u64;
typedef unsigned short u16;
typedef u32 v4u __attribute__((ext_vector_type(4)));
typedef u32 v2u __attribute__((ext_vector_type(2)));
typedef float v4f __attribute__((ext_vector_type(4)));
typedef float v2f __attribute__((ext_vector_type(2)));
typedef short s8v __attribute__((ext_vector_type(8)));
typedef short s4v __attribute__((ext_vector_type(4)));
typedef __bf16 bf2v __attribute__((ext_vector_type(2)));
#define LAS __attribute__((address_space(3)))
#define CAS __attribute__((address_space(4)))

#define KV_NT " nt"
#define W_NT " nt"
#define DLY1 6
#define DLY2 6
#define SPIN_CAP 200000

constexpr u32 WS_QP = 4096;
constexpr u32 WS_HP = WS_QP + 32 * 8 * 2 * 1024;
constexpr u32 WS_OP = WS_HP + 43 * 4 * 512 + 2048;
constexpr u32 WS_SP = WS_OP + 256 * 1152 + 4096;
static_assert(WS_HP % 128 == 0 && WS_OP % 128 == 0 && WS_SP % 128 == 0, "line aligned");
constexpr u32 L_WQ = 0;
constexpr u32 L_W1 = 16384;
constexpr u32 L_WO = 24576;
constexpr u32 L_W2 = 40960;
constexpr u32 L_XN = 49152;
constexpr u32 L_QB = 51200;
constexpr u32 L_HB = 51456;
constexpr u32 L_ON = 51712;
constexpr u32 L_MX = 52224;
constexpr u32 L_SM = 52256;
constexpr u32 L_RED = 52352;
constexpr u32 L_BYTES = 86016;

__device__ __forceinline__ v4f mfma16(v4u a, v4u b, v4f c) {
  return __builtin_amdgcn_mfma_f32_16x16x32_bf16(__builtin_bit_cast(s8v, a), __builtin_bit_cast(s8v, b), c, 0, 0, 0);
}
__device__ __forceinline__ u32 pk2(float lo, float hi) { bf2v z = {(__bf16)lo, (__bf16)hi}; return __builtin_bit_cast(u32, z); }
__device__ __forceinline__ float rbf(float x) { return __uint_as_float(pk2(x, x) & 0xffff0000u); }
__device__ __forceinline__ float bflo(u32 v) { return __uint_as_float(v << 16); }
__device__ __forceinline__ float bfhi(u32 v) { return __uint_as_float(v & 0xffff0000u); }
template <int CTRL> __device__ __forceinline__ float dppf(float x) {
  return __builtin_bit_cast(float, __builtin_amdgcn_mov_dpp(__builtin_bit_cast(int, x), CTRL, 0xf, 0xf, true));
}
template <int CTRL> __device__ __forceinline__ u32 dppu(u32 x) { return (u32)__builtin_amdgcn_mov_dpp((int)x, CTRL, 0xf, 0xf, true); }
constexpr int XOR1 = 0xB1, XOR2 = 0x4E, HMIR = 0x141, MIR = 0x140, ROR4 = 0x124, ROR8 = 0x128, BC0 = 0x150;
__device__ __forceinline__ float x16sum(float x) {
  auto s = __builtin_amdgcn_permlane16_swap(__float_as_uint(x), __float_as_uint(x), false, false);
  x = __uint_as_float(s[0]) + __uint_as_float(s[1]);
  auto t = __builtin_amdgcn_permlane32_swap(__float_as_uint(x), __float_as_uint(x), false, false);
  return __uint_as_float(t[0]) + __uint_as_float(t[1]);
}
__device__ __forceinline__ float x16max(float x) {
  auto s = __builtin_amdgcn_permlane16_swap(__float_as_uint(x), __float_as_uint(x), false, false);
  x = fmaxf(__uint_as_float(s[0]), __uint_as_float(s[1]));
  auto t = __builtin_amdgcn_permlane32_swap(__float_as_uint(x), __float_as_uint(x), false, false);
  return fmaxf(__uint_as_float(t[0]), __uint_as_float(t[1]));
}
__device__ __forceinline__ float wave_sum(float x) {
  x += dppf<XOR1>(x); x += dppf<XOR2>(x); x += dppf<HMIR>(x); x += dppf<MIR>(x);
  return x16sum(x);
}
#define BARRIER() asm volatile("s_waitcnt lgkmcnt(0)\n\ts_barrier" ::: "memory")
#define VMWAIT(n) asm volatile("s_waitcnt vmcnt(" #n ")" ::: "memory")

#define DMA16(voff, base, lds, POL) \
  asm volatile("s_mov_b32 m0, %2\n\ts_nop 0\n\tglobal_load_lds_dwordx4 %0, %1" POL :: "v"(voff), "s"(base), "s"(lds) : "memory")
#define LD16(dst, voff, base, imm, POL, NAME) \
  asm volatile("global_load_dwordx4 %0, %1, %2 offset:" #imm POL " ; LD " NAME : "=v"(dst) : "v"(voff), "s"(base) : "memory")

__device__ __forceinline__ v4u lds128(const LAS unsigned char* L, u32 off) { return *(const LAS v4u*)(L + off); }
__device__ __forceinline__ v4u trfrag(const LAS unsigned char* L, u32 off, u32 step4) {
  s4v lo = __builtin_amdgcn_ds_read_tr16_b64_v4i16((LAS s4v*)(L + off));
  s4v hi = __builtin_amdgcn_ds_read_tr16_b64_v4i16((LAS s4v*)(L + off + step4));
  v2u a = __builtin_bit_cast(v2u, lo), b = __builtin_bit_cast(v2u, hi);
  return v4u{a.x, a.y, b.x, b.y};
}

#define STAMP(i) do {} while (0)

__global__ void __launch_bounds__(512, 2)
fused_decode(const u16* __restrict__ x, const u16* __restrict__ kc, const u16* __restrict__ vc, const u16* __restrict__ wq,
             const u16* __restrict__ wo, const u16* __restrict__ w1, const u16* __restrict__ b1, const u16* __restrict__ w2,
             const u16* __restrict__ b2, u16* __restrict__ out, unsigned char* __restrict__ ws) {
  __shared__ __attribute__((aligned(16))) unsigned char smem[L_BYTES];
  LAS unsigned char* L = (LAS unsigned char*)smem;
  const u32 ldsb = (u32)(size_t)smem;
  STAMP(0);
  u64 pt0; asm volatile("s_memrealtime %0\n\ts_waitcnt lgkmcnt(0)" : "=s"(pt0) :: "memory");
  asm volatile("" :: "s"(x), "s"(kc), "s"(vc), "s"(wq), "s"(wo), "s"(w1), "s"(b1), "s"(w2), "s"(b2), "s"(out), "s"(ws));
  const u32 tid = threadIdx.x, lane = tid & 63;
  const u32 wv = __builtin_amdgcn_readfirstlane(tid >> 6);
  const u32 w = blockIdx.x, xcd = w & 7, r = w >> 3;
  const u32 h = 4 * xcd + (r >> 3), j = r & 7, b = j >> 2, sp = j & 3;
  const u32 g = lane >> 4, i16 = lane & 15;
  const u32 cmb = (r >> 1) * 8 + xcd, kap = 2 * (cmb >> 2) + (r & 1), kq = cmb & 3;
  const bool ffn = (r < 22) && (kap < 43);
  const u32 nk = r < 21 ? 1 : 2, kap2 = r < 21 ? r : 21 + 2 * (r - 21);
  const u32 tag = *(const CAS u32*)ws + 1u;

  v4u xr;
  if (wv < 2) { const u32 vo = wv * 1024 + lane * 16; LD16(xr, vo, x, 0, "", "X"); }
  {
    const u32 vo = (64 * j + 8 * wv + g) * 8192 + h * 256 + i16 * 16;
    DMA16(vo, wq, ldsb + L_WQ + wv * 2048, W_NT);
    const u32 vo2 = vo + 4 * 8192;
    DMA16(vo2, wq, ldsb + L_WQ + wv * 2048 + 1024, W_NT);
  }
  if (ffn) {
    const u32 vo = (128 * kq + 16 * wv + (lane >> 2)) * 2752 + kap * 64 + (lane & 3) * 16;
    DMA16(vo, w1, ldsb + L_W1 + wv * 1024, W_NT);
  }
  const u32 kvbase = ((b * 32 + h) * 4096 + 3072 + sp * 256 + wv * 32) * 256 + 4096;
  const u32 vK = kvbase + ((lane >> 2) & 3) * 256 + (4 * g + (lane & 3)) * 16;
  const u32 vV = kvbase + g * 1024 + i16 * 16;
  v4u K[8], V[8];
  LD16(K[0], vK, kc, -4096, KV_NT, "K0");
  LD16(K[1], vK, kc, -3072, KV_NT, "K1");
  LD16(K[2], vK, kc, -2048, KV_NT, "K2");
  LD16(K[3], vK, kc, -1024, KV_NT, "K3");
  STAMP(1);
  if (wv < 2) {
    asm volatile("s_waitcnt vmcnt(6) ; GUARD X" : "+v"(xr) :: "memory");
    float v[8] = {bflo(xr.x), bfhi(xr.x), bflo(xr.y), bfhi(xr.y), bflo(xr.z), bfhi(xr.z), bflo(xr.w), bfhi(xr.w)};
    float s = ((v[0] + v[1]) + (v[2] + v[3])) + ((v[4] + v[5]) + (v[6] + v[7]));
    const float mean = wave_sum(s) * (1.f / 512.f);
    float q = 0.f;
#pragma unroll
    for (int k = 0; k < 8; k++) { const float d = v[k] - mean; q = fmaf(d, d, q); }
    const float var = wave_sum(q) * (1.f / 512.f);
    const float m = rbf(mean);
    const float sd = rbf(__builtin_amdgcn_sqrtf(rbf(rbf(var) + 1e-5f)));
    const float inv = __builtin_amdgcn_rcpf(sd);
    v4u o;
    o.x = pk2(rbf(v[0] - m) * inv, rbf(v[1] - m) * inv);
    o.y = pk2(rbf(v[2] - m) * inv, rbf(v[3] - m) * inv);
    o.z = pk2(rbf(v[4] - m) * inv, rbf(v[5] - m) * inv);
    o.w = pk2(rbf(v[6] - m) * inv, rbf(v[7] - m) * inv);
    *(LAS v4u*)(L + L_XN + wv * 1024 + lane * 16) = o;
  }
  VMWAIT(4);
  BARRIER();
  STAMP(2);
  const u32 trq = (i16 >> 2), trp = lane & 3;
  {
    const u32 aoff = L_XN + (lane & 1) * 1024 + (64 * j + 8 * g) * 2;
    const u32 boff = L_WQ + (8 * g + trq) * 256 + wv * 32 + trp * 8;
    v4f d = {0.f, 0.f, 0.f, 0.f};
    d = mfma16(lds128(L, aoff), trfrag(L, boff, 1024), d);
    d = mfma16(lds128(L, aoff + 64), trfrag(L, boff + 32 * 256, 1024), d);
    if (lane < 16) {
      const u32 vo = WS_QP + (h * 8 + j) * 2048 + (16 * wv + lane) * 8;
      const v2u g0 = {__float_as_uint(d[0]), tag}, g1 = {__float_as_uint(d[1]), tag};
      asm volatile("global_store_dwordx2 %0, %1, %3 sc1\n\tglobal_store_dwordx2 %0, %2, %3 offset:1024 sc1" :: "v"(vo), "v"(g0), "v"(g1), "s"(ws) : "memory");
    }
  }
  if (ffn && (wv == 2 || wv == 3)) {
    const u32 c = wv - 2;
    const u32 aoff = L_XN + (lane & 1) * 1024 + (128 * kq + 8 * g) * 2;
    const u32 boff = L_W1 + (8 * g + trq) * 64 + c * 32 + trp * 8;
    v4f d = {0.f, 0.f, 0.f, 0.f};
#pragma unroll
    for (int ks = 0; ks < 4; ks++) d = mfma16(lds128(L, aoff + ks * 64), trfrag(L, boff + ks * 32 * 64, 256), d);
    if (lane < 16) {
      const u32 vo = WS_HP + (kap * 4 + kq) * 512 + (16 * c + lane) * 8;
      const v2u g0 = {__float_as_uint(d[0]), tag}, g1 = {__float_as_uint(d[1]), tag};
      asm volatile("global_store_dwordx2 %0, %1, %3 sc1\n\tglobal_store_dwordx2 %0, %2, %3 offset:256 sc1" :: "v"(vo), "v"(g0), "v"(g1), "s"(ws) : "memory");
    }
  }
  LD16(K[4], vK, kc, 0, KV_NT, "K4");
  LD16(K[5], vK, kc, 1024, KV_NT, "K5");
  LD16(K[6], vK, kc, 2048, KV_NT, "K6");
  LD16(K[7], vK, kc, 3072, KV_NT, "K7");
  LD16(V[0], vV, vc, -4096, KV_NT, "V0");
  LD16(V[1], vV, vc, -3840, KV_NT, "V1");
  LD16(V[2], vV, vc, -3584, KV_NT, "V2");
  LD16(V[3], vV, vc, -3328, KV_NT, "V3");
  LD16(V[4], vV, vc, 0, KV_NT, "V4");
  LD16(V[5], vV, vc, 256, KV_NT, "V5");
  LD16(V[6], vV, vc, 512, KV_NT, "V6");
  LD16(V[7], vV, vc, 768, KV_NT, "V7");
  STAMP(3);
  if (wv == 0) {
    VMWAIT(8);
    const u32 vo = WS_QP + h * 8 * 2048 + b * 1024 + lane * 16 + 4096, vo2 = vo + 8192;
    float q0, q1;
    for (u32 spin = 0;; spin++) {
      v4u p0, p1, p2, p3, p4, p5, p6, p7;
      asm volatile(
          "global_load_dwordx4 %0, %8, %10 offset:-4096 sc1\n\t"
          "global_load_dwordx4 %1, %8, %10 offset:-2048 sc1\n\t"
          "global_load_dwordx4 %2, %8, %10 sc1\n\t"
          "global_load_dwordx4 %3, %8, %10 offset:2048 sc1\n\t"
          "global_load_dwordx4 %4, %9, %10 offset:-4096 sc1\n\t"
          "global_load_dwordx4 %5, %9, %10 offset:-2048 sc1\n\t"
          "global_load_dwordx4 %6, %9, %10 sc1\n\t"
          "global_load_dwordx4 %7, %9, %10 offset:2048 sc1\n\t"
          "s_waitcnt vmcnt(0)"
          : "=&v"(p0), "=&v"(p1), "=&v"(p2), "=&v"(p3), "=&v"(p4), "=&v"(p5), "=&v"(p6), "=&v"(p7)
          : "v"(vo), "v"(vo2), "s"(ws) : "memory");
      const u32 bad = ((p0.y ^ tag) | (p0.w ^ tag) | (p1.y ^ tag) | (p1.w ^ tag)) | ((p2.y ^ tag) | (p2.w ^ tag) | (p3.y ^ tag) | (p3.w ^ tag)) |
                      ((p4.y ^ tag) | (p4.w ^ tag) | (p5.y ^ tag) | (p5.w ^ tag)) | ((p6.y ^ tag) | (p6.w ^ tag) | (p7.y ^ tag) | (p7.w ^ tag));
      q0 = ((__uint_as_float(p0.x) + __uint_as_float(p1.x)) + (__uint_as_float(p2.x) + __uint_as_float(p3.x))) +
           ((__uint_as_float(p4.x) + __uint_as_float(p5.x)) + (__uint_as_float(p6.x) + __uint_as_float(p7.x)));
      q1 = ((__uint_as_float(p0.z) + __uint_as_float(p1.z)) + (__uint_as_float(p2.z) + __uint_as_float(p3.z))) +
           ((__uint_as_float(p4.z) + __uint_as_float(p5.z)) + (__uint_as_float(p6.z) + __uint_as_float(p7.z)));
      if (__all(bad == 0) || spin > SPIN_CAP) break;
      __builtin_amdgcn_s_sleep(2);
    }
    *(LAS u32*)(L + L_QB + lane * 4) = pk2(q0, q1);
    STAMP(4);
  } else if (wv == 1) {
    VMWAIT(8);
    const u32 bb = lane >> 5, f = lane & 31;
    for (u32 ki = 0; ki < nk; ki++) {
      const u32 kp = kap2 + ki;
      const u32 vo = WS_HP + kp * 2048 + bb * 256 + f * 8;
      const u32 vb = (kp * 32 + f) * 2;
      float hs; u32 bias;
      for (u32 spin = 0;; spin++) {
        v2u p0, p1, p2, p3;
        asm volatile(
            "global_load_ushort %4, %6, %8\n\t"
            "global_load_dwordx2 %0, %5, %7 sc1\n\t"
            "global_load_dwordx2 %1, %5, %7 offset:512 sc1\n\t"
            "global_load_dwordx2 %2, %5, %7 offset:1024 sc1\n\t"
            "global_load_dwordx2 %3, %5, %7 offset:1536 sc1\n\t"
            "s_waitcnt vmcnt(0)"
            : "=&v"(p0), "=&v"(p1), "=&v"(p2), "=&v"(p3), "=&v"(bias)
            : "v"(vo), "v"(vb), "s"(ws), "s"(b1) : "memory");
        const u32 bad = (p0.y ^ tag) | (p1.y ^ tag) | (p2.y ^ tag) | (p3.y ^ tag);
        hs = (__uint_as_float(p0.x) + __uint_as_float(p1.x)) + (__uint_as_float(p2.x) + __uint_as_float(p3.x));
        if (__all(bad == 0) || spin > SPIN_CAP) break;
        __builtin_amdgcn_s_sleep(2);
      }
      float hv = rbf(rbf(hs) + bflo(bias));
      hv = hv > 0.f ? hv : 0.f;
      *(LAS u16*)(L + L_HB + bb * 128 + ki * 64 + f * 2) = (u16)pk2(hv * hv, 0.f);
    }
  }
  BARRIER();
  STAMP(5);
  float t0, t1;
  {
    const v4u qc = lds128(L, L_QB + (4 * g + (lane & 3)) * 16);
    v4u A[4];
#pragma unroll
    for (int a = 0; a < 4; a++) {
      const bool on = (i16 >> 2) == (u32)a;
      A[a] = v4u{on ? qc.x : 0u, on ? qc.y : 0u, on ? qc.z : 0u, on ? qc.w : 0u};
    }
    asm volatile("s_waitcnt vmcnt(8) ; GUARD K0 K1 K2 K3 K4 K5 K6 K7" : "+v"(K[0]), "+v"(K[1]), "+v"(K[2]), "+v"(K[3]), "+v"(K[4]), "+v"(K[5]), "+v"(K[6]), "+v"(K[7]) :: "memory");
    v4f d0 = {0.f, 0.f, 0.f, 0.f}, d1 = {0.f, 0.f, 0.f, 0.f};
#pragma unroll
    for (int a = 0; a < 4; a++) { d0 = mfma16(A[a], K[a], d0); d1 = mfma16(A[a], K[4 + a], d1); }
    const bool s1 = lane & 1, s2 = lane & 2;
    float u0 = s2 ? (s1 ? d0[3] : d0[2]) : (s1 ? d0[1] : d0[0]);
    float u1 = s2 ? (s1 ? d1[3] : d1[2]) : (s1 ? d1[1] : d1[0]);
    u0 += dppf<XOR1>(u0); u1 += dppf<XOR1>(u1);
    u0 += dppf<XOR2>(u0); u1 += dppf<XOR2>(u1);
    constexpr float SC = 0.08838834764831845f, L2E = 1.4426950408889634f;
    t0 = rbf(rbf(u0) * SC) * L2E;
    t1 = rbf(rbf(u1) * SC) * L2E;
  }
  {
    float m = fmaxf(t0, t1);
    m = fmaxf(m, dppf<ROR4>(m)); m = fmaxf(m, dppf<ROR8>(m));
    m = x16max(m);
    if (lane == 0) *(LAS float*)(L + L_MX + wv * 4) = m;
  }
  BARRIER();
  STAMP(6);
  {
    const u32 vo = (r * 128 + 16 * wv + (lane >> 3)) * 1024 + xcd * 128 + (lane & 7) * 16;
    DMA16(vo, wo, ldsb + L_WO + wv * 2048, W_NT);
    const u32 vo2 = vo + 8 * 1024;
    DMA16(vo2, wo, ldsb + L_WO + wv * 2048 + 1024, W_NT);
    const u32 lw2 = __builtin_amdgcn_readfirstlane(ldsb + L_W2 + wv * 512 * nk);
    const u32 vw = (kap2 * 32 + wv * 4 * nk + (lane >> 3)) * 1024 + xcd * 128 + (lane & 7) * 16;
    asm volatile("" :: "s"(lw2));
    if (lane < 32 * nk) {
      DMA16(vw, w2, lw2, W_NT);
    }
  }
  float M;
  v4u AV[8];
  {
    const v4f ma = *(const LAS v4f*)(L + L_MX), mb = *(const LAS v4f*)(L + L_MX + 16);
    M = fmaxf(fmaxf(fmaxf(ma[0], ma[1]), fmaxf(ma[2], ma[3])), fmaxf(fmaxf(mb[0], mb[1]), fmaxf(mb[2], mb[3])));
    const float p0 = __builtin_amdgcn_exp2f(t0 - M), p1 = __builtin_amdgcn_exp2f(t1 - M);
    float ls = p0 + p1;
    ls += dppf<ROR4>(ls); ls += dppf<ROR8>(ls);
    ls = x16sum(ls);
    if (lane == 0) *(LAS float*)(L + L_SM + wv * 4) = ls;
    const u32 pp0 = pk2(p0, p0), pp1 = pk2(p1, p1);
    const u32 hm = (lane & 1) ? 0xffff0000u : 0x0000ffffu, kd = (lane & 7) >> 1;
    const u32 mk0 = kd == 0 ? hm : 0u, mk1 = kd == 1 ? hm : 0u, mk2 = kd == 2 ? hm : 0u, mk3 = kd == 3 ? hm : 0u;
#define AVB(t, pp, c) { const u32 z = dppu<BC0 + c>(pp); AV[t] = v4u{z & mk0, z & mk1, z & mk2, z & mk3}; }
    AVB(0, pp0, 0) AVB(1, pp0, 4) AVB(2, pp0, 8) AVB(3, pp0, 12) AVB(4, pp1, 0) AVB(5, pp1, 4) AVB(6, pp1, 8) AVB(7, pp1, 12)
#undef AVB
    asm volatile("" : "+v"(AV[0]), "+v"(AV[1]), "+v"(AV[2]), "+v"(AV[3]), "+v"(AV[4]), "+v"(AV[5]), "+v"(AV[6]), "+v"(AV[7]));
  }
  {
    v4f d = {0.f, 0.f, 0.f, 0.f};
#define PVS(t, n) asm volatile("s_waitcnt vmcnt(" #n ") ; GUARD V" #t : "+v"(V[t]) :: "memory"); d = mfma16(AV[t], V[t], d); __builtin_amdgcn_sched_barrier(0);
    PVS(0, 10) PVS(1, 9) PVS(2, 8) PVS(3, 7) PVS(4, 6) PVS(5, 5) PVS(6, 4) PVS(7, 3)
#undef PVS
    if (lane < 32) *(LAS v4f*)(L + L_RED + wv * 512 + (8 * i16 + 4 * g) * 4) = d;
  }
  BARRIER();
  STAMP(7);
  v4u P0, P1, P2, P3, P4, P5, P6, P7, PM;
  const u32 voP = WS_OP + r * 8 * 1152 + lane * 16 + 4032, voM = WS_OP + (r * 8 + (lane & 7)) * 1152 + 1024;
  if (wv == 0) {
    float o0 = 0.f, o1 = 0.f;
#pragma unroll
    for (int k = 0; k < 8; k++) { const v2f v = *(const LAS v2f*)(L + L_RED + k * 512 + lane * 8); o0 += v.x; o1 += v.y; }
    const u32 vo = WS_OP + (h * 8 + j) * 1152 + lane * 16;
    const v4u gq = {__float_as_uint(o0), tag, __float_as_uint(o1), tag};
    asm volatile("global_store_dwordx4 %0, %1, %2 sc1\n\ts_nop 1" :: "v"(vo), "v"(gq), "s"(ws) : "memory");
    VMWAIT(1);
  } else if (wv == 1) {
    const v4f sa = *(const LAS v4f*)(L + L_SM), sb = *(const LAS v4f*)(L + L_SM + 16);
    const float ls = (((sa[0] + sa[1]) + (sa[2] + sa[3])) + ((sb[0] + sb[1]) + (sb[2] + sb[3])));
    if (lane == 0) {
      const u32 vo = WS_OP + (h * 8 + j) * 1152 + 1024;
      const v4u gq = {__float_as_uint(M), tag, __float_as_uint(ls), tag};
      asm volatile("global_store_dwordx4 %0, %1, %2 sc1\n\ts_nop 1" :: "v"(vo), "v"(gq), "s"(ws) : "memory");
    }
    VMWAIT(1);
  } else if (wv == 2) {
    __builtin_amdgcn_s_sleep(DLY1);
    asm volatile(
        "global_load_dwordx4 %8, %10, %11 sc1 ; LD PM\n\t"
        "global_load_dwordx4 %0, %9, %11 offset:-4032 sc1 ; LD P0\n\t"
        "global_load_dwordx4 %1, %9, %11 offset:-2880 sc1 ; LD P1\n\t"
        "global_load_dwordx4 %2, %9, %11 offset:-1728 sc1 ; LD P2\n\t"
        "global_load_dwordx4 %3, %9, %11 offset:-576 sc1 ; LD P3\n\t"
        "global_load_dwordx4 %4, %9, %11 offset:576 sc1 ; LD P4\n\t"
        "global_load_dwordx4 %5, %9, %11 offset:1728 sc1 ; LD P5\n\t"
        "global_load_dwordx4 %6, %9, %11 offset:2880 sc1 ; LD P6\n\t"
        "global_load_dwordx4 %7, %9, %11 offset:4032 sc1 ; LD P7\n\t"
        "s_waitcnt vmcnt(9)"
        : "=&v"(P0), "=&v"(P1), "=&v"(P2), "=&v"(P3), "=&v"(P4), "=&v"(P5), "=&v"(P6), "=&v"(P7), "=&v"(PM)
        : "v"(voP), "v"(voM), "s"(ws) : "memory");
  } else {
    VMWAIT(0);
  }
  BARRIER();
  STAMP(8);
  v4u BW[4];
  v4f dg = {0.f, 0.f, 0.f, 0.f};
  if (wv >= 4) {
    const u32 t = wv - 4;
    const u32 boff = (8 * g + trq) * 128 + t * 32 + trp * 8;
#pragma unroll
    for (int ks = 0; ks < 4; ks++) BW[ks] = trfrag(L, L_WO + boff + ks * 4096, 512);
    for (u32 ki = 0; ki < nk; ki++)
      dg = mfma16(lds128(L, L_HB + (lane & 1) * 128 + ki * 64 + g * 16), trfrag(L, L_W2 + boff + ki * 4096, 512), dg);
  } else if (wv == 2) {
    asm volatile("s_waitcnt vmcnt(0) ; GUARD P0 P1 P2 P3 P4 P5 P6 P7 PM" : "+v"(P0), "+v"(P1), "+v"(P2), "+v"(P3), "+v"(P4), "+v"(P5), "+v"(P6), "+v"(P7), "+v"(PM) :: "memory");
    v4u p0 = P0, p1 = P1, p2 = P2, p3 = P3, p4 = P4, p5 = P5, p6 = P6, p7 = P7, pm = PM;
    for (u32 spin = 0;; spin++) {
      const u32 bad = ((p0.y ^ tag) | (p0.w ^ tag) | (p1.y ^ tag) | (p1.w ^ tag)) | ((p2.y ^ tag) | (p2.w ^ tag) | (p3.y ^ tag) | (p3.w ^ tag)) |
                      ((p4.y ^ tag) | (p4.w ^ tag) | (p5.y ^ tag) | (p5.w ^ tag)) | ((p6.y ^ tag) | (p6.w ^ tag) | (p7.y ^ tag) | (p7.w ^ tag)) |
                      ((pm.y ^ tag) | (pm.w ^ tag));
      if (__all(bad == 0) || spin > SPIN_CAP) break;
      asm volatile(
          "global_load_dwordx4 %8, %10, %11 sc1\n\t"
          "global_load_dwordx4 %0, %9, %11 offset:-4032 sc1\n\t"
          "global_load_dwordx4 %1, %9, %11 offset:-2880 sc1\n\t"
          "global_load_dwordx4 %2, %9, %11 offset:-1728 sc1\n\t"
          "global_load_dwordx4 %3, %9, %11 offset:-576 sc1\n\t"
          "global_load_dwordx4 %4, %9, %11 offset:576 sc1\n\t"
          "global_load_dwordx4 %5, %9, %11 offset:1728 sc1\n\t"
          "global_load_dwordx4 %6, %9, %11 offset:2880 sc1\n\t"
          "global_load_dwordx4 %7, %9, %11 offset:4032 sc1\n\t"
          "s_waitcnt vmcnt(0)"
          : "=&v"(p0), "=&v"(p1), "=&v"(p2), "=&v"(p3), "=&v"(p4), "=&v"(p5), "=&v"(p6), "=&v"(p7), "=&v"(pm)
          : "v"(voP), "v"(voM), "s"(ws) : "memory");
    }
    STAMP(9);
    const float mp = __uint_as_float(pm.x), lp = __uint_as_float(pm.z);
    float Mb = fmaxf(mp, dppf<XOR1>(mp)); Mb = fmaxf(Mb, dppf<XOR2>(Mb));
    const float e = __builtin_amdgcn_exp2f(mp - Mb);
    float Lb = e * lp; Lb += dppf<XOR1>(Lb); Lb += dppf<XOR2>(Lb);
    const float f = e * __builtin_amdgcn_rcpf(Lb);
    const float f0 = dppf<BC0 + 0>(f), f1 = dppf<BC0 + 1>(f), f2 = dppf<BC0 + 2>(f), f3 = dppf<BC0 + 3>(f);
    const float f4 = dppf<BC0 + 4>(f), f5 = dppf<BC0 + 5>(f), f6 = dppf<BC0 + 6>(f), f7 = dppf<BC0 + 7>(f);
    const float a0 = fmaf(f0, __uint_as_float(p0.x), f1 * __uint_as_float(p1.x)) + fmaf(f2, __uint_as_float(p2.x), f3 * __uint_as_float(p3.x));
    const float a1 = fmaf(f0, __uint_as_float(p0.z), f1 * __uint_as_float(p1.z)) + fmaf(f2, __uint_as_float(p2.z), f3 * __uint_as_float(p3.z));
    const float c0 = fmaf(f4, __uint_as_float(p4.x), f5 * __uint_as_float(p5.x)) + fmaf(f6, __uint_as_float(p6.x), f7 * __uint_as_float(p7.x));
    const float c1 = fmaf(f4, __uint_as_float(p4.z), f5 * __uint_as_float(p5.z)) + fmaf(f6, __uint_as_float(p6.z), f7 * __uint_as_float(p7.z));
    *(LAS u32*)(L + L_ON + lane * 4) = pk2(a0, a1);
    *(LAS u32*)(L + L_ON + 256 + lane * 4) = pk2(c0, c1);
  }
  BARRIER();
  STAMP(10);
  if (wv >= 4) {
    const u32 t = wv - 4;
    const u32 aoff = L_ON + (lane & 1) * 256 + g * 16;
    v4f d = {0.f, 0.f, 0.f, 0.f};
#pragma unroll
    for (int ks = 0; ks < 4; ks++) d = mfma16(lds128(L, aoff + ks * 64), BW[ks], d);
    auto ss = __builtin_amdgcn_permlane16_swap(__float_as_uint(d[0]), __float_as_uint(d[1]), false, false);
    auto gg = __builtin_amdgcn_permlane16_swap(__float_as_uint(dg[0]), __float_as_uint(dg[1]), false, false);
    if (lane < 32) {
      const u32 vo = WS_SP + ((xcd * 32 + r) * 128 + g * 64 + 16 * t + i16) * 16;
      const v4u gq = {ss[0], tag, gg[0], tag};
      asm volatile("global_store_dwordx4 %0, %1, %2 sc1\n\ts_nop 1" :: "v"(vo), "v"(gq), "s"(ws) : "memory");
    }
    STAMP(11);
  } else if (wv == 0 && r < 16) {
    const u32 bb = r >> 3, col = xcd * 64 + (r & 7) * 8 + (lane & 7);
    const u32 vo = WS_SP + ((xcd * 32 + (lane >> 3)) * 128 + bb * 64 + (r & 7) * 8 + (lane & 7)) * 16;
    const u32 vo1 = vo + 8 * 2048, vo2 = vo + 16 * 2048, vo3 = vo + 24 * 2048;
    const u32 vx = (bb * 512 + col) * 2, vb = col * 2;
    u32 xv, bv;
    asm volatile("global_load_ushort %0, %2, %4 ; LD XV\n\tglobal_load_ushort %1, %3, %5 ; LD BV" : "=&v"(xv), "=&v"(bv) : "v"(vx), "v"(vb), "s"(x), "s"(b2) : "memory");
    __builtin_amdgcn_s_sleep(DLY2);
    float s, gs;
    for (u32 spin = 0;; spin++) {
      v4u p0, p1, p2, p3;
      asm volatile(
          "global_load_dwordx4 %0, %4, %8 sc1\n\t"
          "global_load_dwordx4 %1, %5, %8 sc1\n\t"
          "global_load_dwordx4 %2, %6, %8 sc1\n\t"
          "global_load_dwordx4 %3, %7, %8 sc1\n\t"
          "s_waitcnt vmcnt(0)"
          : "=&v"(p0), "=&v"(p1), "=&v"(p2), "=&v"(p3)
          : "v"(vo), "v"(vo1), "v"(vo2), "v"(vo3), "s"(ws) : "memory");
      const u32 bad = ((p0.y ^ tag) | (p0.w ^ tag) | (p1.y ^ tag) | (p1.w ^ tag)) | ((p2.y ^ tag) | (p2.w ^ tag) | (p3.y ^ tag) | (p3.w ^ tag));
      s = (__uint_as_float(p0.x) + __uint_as_float(p1.x)) + (__uint_as_float(p2.x) + __uint_as_float(p3.x));
      gs = (__uint_as_float(p0.z) + __uint_as_float(p1.z)) + (__uint_as_float(p2.z) + __uint_as_float(p3.z));
      if (__all(bad == 0) || spin > SPIN_CAP) break;
    }
    asm volatile("; GUARD XV BV" : "+v"(xv), "+v"(bv));
    s += dppf<ROR8>(s); gs += dppf<ROR8>(gs);
    s = x16sum(s); gs = x16sum(gs);
    const float gt = rbf(rbf(gs) + bflo(bv));
    const float sg = rbf(gt * __builtin_amdgcn_rcpf(1.f + __builtin_amdgcn_exp2f(gt * -1.4426950408889634f)));
    const float o = bflo(xv) + rbf(rbf(s) * sg);
    if (lane < 8) {
      const u32 vo_ = (bb * 512 + col) * 2;
      u32 ob = pk2(o, o);
      { u64 pt1; asm volatile("s_memrealtime %0\n\ts_waitcnt lgkmcnt(0)" : "=s"(pt1) :: "memory"); if (w == 0 && lane == 0 && (pt1 - pt0) > PROBE_TH) ob ^= 1u; }
      asm volatile("global_store_short %0, %1, %2" :: "v"(vo_), "v"(ob), "s"(out) : "memory");
    }
    if (w == 0 && lane == 0) {
      const u32 z = 0;
      asm volatile("global_store_dword %0, %1, %2 sc1" :: "v"(z), "v"(tag), "s"(ws) : "memory");
    }
    STAMP(12);
  }
}

extern "C" void kernel_launch(void* const* d_in, const int* in_sizes, int n_in, void* d_out, int out_size, void* d_ws, size_t ws_size,
                              hipStream_t stream) {
  fused_decode<<<256, 512, 0, stream>>>((const u16*)d_in[0], (const u16*)d_in[1], (const u16*)d_in[2], (const u16*)d_in[3], (const u16*)d_in[4],
                                        (const u16*)d_in[6], (const u16*)d_in[7], (const u16*)d_in[8], (const u16*)d_in[9], (u16*)d_out,
                                        (unsigned char*)d_ws);
}
```

```cpp
#define STAMPS
#define PROBE_IDX 0
#include <hip/hip_runtime.h>
#include <stdint.h>

typedef unsigned u32;
typedef unsigned long long u64;
typedef unsigned short u16;
typedef u32 v4u __attribute__((ext_vector_type(4)));
typedef u32 v2u __attribute__((ext_vector_type(2)));
typedef float v4f __attribute__((ext_vector_type(4)));
typedef float v2f __attribute__((ext_vector_type(2)));
typedef short s8v __attribute__((ext_vector_type(8)));
typedef short s4v __attribute__((ext_vector_type(4)));
typedef __bf16 bf2v __attribute__((ext_vector_type(2)));
#define LAS __attribute__((address_space(3)))
#define CAS __attribute__((address_space(4)))

#define KV_NT " nt"
#define W_NT " nt"
#define DLY1 6
#define DLY2 6
#define SPIN_CAP 200000

constexpr u32 WS_QP = 4096;
constexpr u32 WS_HP = WS_QP + 32 * 8 * 2 * 1024;
constexpr u32 WS_OP = WS_HP + 43 * 4 * 512 + 2048;
constexpr u32 WS_SP = WS_OP + 256 * 1152 + 4096;
static_assert(WS_HP % 128 == 0 && WS_OP % 128 == 0 && WS_SP % 128 == 0, "line aligned");
constexpr u32 L_WQ = 0;
constexpr u32 L_W1 = 16384;
constexpr u32 L_WO = 24576;
constexpr u32 L_W2 = 40960;
constexpr u32 L_XN = 49152;
constexpr u32 L_QB = 51200;
constexpr u32 L_HB = 51456;
constexpr u32 L_ON = 51712;
constexpr u32 L_MX = 52224;
constexpr u32 L_SM = 52256;
constexpr u32 L_RED = 52352;
constexpr u32 L_BYTES = 86016;

__device__ __forceinline__ v4f mfma16(v4u a, v4u b, v4f c) {
  return __builtin_amdgcn_mfma_f32_16x16x32_bf16(__builtin_bit_cast(s8v, a), __builtin_bit_cast(s8v, b), c, 0, 0, 0);
}
__device__ __forceinline__ u32 pk2(float lo, float hi) { bf2v z = {(__bf16)lo, (__bf16)hi}; return __builtin_bit_cast(u32, z); }
__device__ __forceinline__ float rbf(float x) { return __uint_as_float(pk2(x, x) & 0xffff0000u); }
__device__ __forceinline__ float bflo(u32 v) { return __uint_as_float(v << 16); }
__device__ __forceinline__ float bfhi(u32 v) { return __uint_as_float(v & 0xffff0000u); }
template <int CTRL> __device__ __forceinline__ float dppf(float x) {
  return __builtin_bit_cast(float, __builtin_amdgcn_mov_dpp(__builtin_bit_cast(int, x), CTRL, 0xf, 0xf, true));
}
template <int CTRL> __device__ __forceinline__ u32 dppu(u32 x) { return (u32)__builtin_amdgcn_mov_dpp((int)x, CTRL, 0xf, 0xf, true); }
constexpr int XOR1 = 0xB1, XOR2 = 0x4E, HMIR = 0x141, MIR = 0x140, ROR4 = 0x124, ROR8 = 0x128, BC0 = 0x150;
__device__ __forceinline__ float x16sum(float x) {
  auto s = __builtin_amdgcn_permlane16_swap(__float_as_uint(x), __float_as_uint(x), false, false);
  x = __uint_as_float(s[0]) + __uint_as_float(s[1]);
  auto t = __builtin_amdgcn_permlane32_swap(__float_as_uint(x), __float_as_uint(x), false, false);
  return __uint_as_float(t[0]) + __uint_as_float(t[1]);
}
__device__ __forceinline__ float x16max(float x) {
  auto s = __builtin_amdgcn_permlane16_swap(__float_as_uint(x), __float_as_uint(x), false, false);
  x = fmaxf(__uint_as_float(s[0]), __uint_as_float(s[1]));
  auto t = __builtin_amdgcn_permlane32_swap(__float_as_uint(x), __float_as_uint(x), false, false);
  return fmaxf(__uint_as_float(t[0]), __uint_as_float(t[1]));
}
__device__ __forceinline__ float wave_sum(float x) {
  x += dppf<XOR1>(x); x += dppf<XOR2>(x); x += dppf<HMIR>(x); x += dppf<MIR>(x);
  return x16sum(x);
}
#define BARRIER() asm volatile("s_waitcnt lgkmcnt(0)\n\ts_barrier" ::: "memory")
#define VMWAIT(n) asm volatile("s_waitcnt vmcnt(" #n ")" ::: "memory")

#define DMA16(voff, base, lds, POL) \
  asm volatile("s_mov_b32 m0, %2\n\ts_nop 0\n\tglobal_load_lds_dwordx4 %0, %1" POL :: "v"(voff), "s"(base), "s"(lds) : "memory")
#define LD16(dst, voff, base, imm, POL, NAME) \
  asm volatile("global_load_dwordx4 %0, %1, %2 offset:" #imm POL " ; LD " NAME : "=v"(dst) : "v"(voff), "s"(base) : "memory")

__device__ __forceinline__ v4u lds128(const LAS unsigned char* L, u32 off) { return *(const LAS v4u*)(L + off); }
__device__ __forceinline__ v4u trfrag(const LAS unsigned char* L, u32 off, u32 step4) {
  s4v lo = __builtin_amdgcn_ds_read_tr16_b64_v4i16((LAS s4v*)(L + off));
  s4v hi = __builtin_amdgcn_ds_read_tr16_b64_v4i16((LAS s4v*)(L + off + step4));
  v2u a = __builtin_bit_cast(v2u, lo), b = __builtin_bit_cast(v2u, hi);
  return v4u{a.x, a.y, b.x, b.y};
}

#define STAMP(i) do { __builtin_amdgcn_sched_barrier(0); asm volatile("s_memrealtime %0\n\ts_waitcnt lgkmcnt(0)" : "=s"(ts[i]) :: "memory"); __builtin_amdgcn_sched_barrier(0); } while (0)

__global__ void __launch_bounds__(512, 2)
fused_decode(const u16* __restrict__ x, const u16* __restrict__ kc, const u16* __restrict__ vc, const u16* __restrict__ wq,
             const u16* __restrict__ wo, const u16* __restrict__ w1, const u16* __restrict__ b1, const u16* __restrict__ w2,
             const u16* __restrict__ b2, u16* __restrict__ out, unsigned char* __restrict__ ws) {
  __shared__ __attribute__((aligned(16))) unsigned char smem[L_BYTES];
  LAS unsigned char* L = (LAS unsigned char*)smem;
  const u32 ldsb = (u32)(size_t)smem;
  u64 ts[16];
#pragma unroll
  for (int i = 0; i < 16; i++) ts[i] = 0;
  STAMP(0);
  asm volatile("" :: "s"(x), "s"(kc), "s"(vc), "s"(wq), "s"(wo), "s"(w1), "s"(b1), "s"(w2), "s"(b2), "s"(out), "s"(ws));
  const u32 tid = threadIdx.x, lane = tid & 63;
  const u32 wv = __builtin_amdgcn_readfirstlane(tid >> 6);
  const u32 w = blockIdx.x, xcd = w & 7, r = w >> 3;
  const u32 h = 4 * xcd + (r >> 3), j = r & 7, b = j >> 2, sp = j & 3;
  const u32 g = lane >> 4, i16 = lane & 15;
  const u32 cmb = (r >> 1) * 8 + xcd, kap = 2 * (cmb >> 2) + (r & 1), kq = cmb & 3;
  const bool ffn = (r < 22) && (kap < 43);
  const u32 nk = r < 21 ? 1 : 2, kap2 = r < 21 ? r : 21 + 2 * (r - 21);
  const u32 tag = *(const CAS u32*)ws + 1u;

  v4u xr;
  if (wv < 2) { const u32 vo = wv * 1024 + lane * 16; LD16(xr, vo, x, 0, "", "X"); }
  {
    const u32 vo = (64 * j + 8 * wv + g) * 8192 + h * 256 + i16 * 16;
    DMA16(vo, wq, ldsb + L_WQ + wv * 2048, W_NT);
    const u32 vo2 = vo + 4 * 8192;
    DMA16(vo2, wq, ldsb + L_WQ + wv * 2048 + 1024, W_NT);
  }
  if (ffn) {
    const u32 vo = (128 * kq + 16 * wv + (lane >> 2)) * 2752 + kap * 64 + (lane & 3) * 16;
    DMA16(vo, w1, ldsb + L_W1 + wv * 1024, W_NT);
  }
  const u32 kvbase = ((b * 32 + h) * 4096 + 3072 + sp * 256 + wv * 32) * 256 + 4096;
  const u32 vK = kvbase + ((lane >> 2) & 3) * 256 + (4 * g + (lane & 3)) * 16;
  const u32 vV = kvbase + g * 1024 + i16 * 16;
  v4u K[8], V[8];
  LD16(K[0], vK, kc, -4096, KV_NT, "K0");
  LD16(K[1], vK, kc, -3072, KV_NT, "K1");
  LD16(K[2], vK, kc, -2048, KV_NT, "K2");
  LD16(K[3], vK, kc, -1024, KV_NT, "K3");
  STAMP(1);
  if (wv < 2) {
    asm volatile("s_waitcnt vmcnt(6) ; GUARD X" : "+v"(xr) :: "memory");
    float v[8] = {bflo(xr.x), bfhi(xr.x), bflo(xr.y), bfhi(xr.y), bflo(xr.z), bfhi(xr.z), bflo(xr.w), bfhi(xr.w)};
    float s = ((v[0] + v[1]) + (v[2] + v[3])) + ((v[4] + v[5]) + (v[6] + v[7]));
    const float mean = wave_sum(s) * (1.f / 512.f);
    float q = 0.f;
#pragma unroll
    for (int k = 0; k < 8; k++) { const float d = v[k] - mean; q = fmaf(d, d, q); }
    const float var = wave_sum(q) * (1.f / 512.f);
    const float m = rbf(mean);
    const float sd = rbf(__builtin_amdgcn_sqrtf(rbf(rbf(var) + 1e-5f)));
    const float inv = __builtin_amdgcn_rcpf(sd);
    v4u o;
    o.x = pk2(rbf(v[0] - m) * inv, rbf(v[1] - m) * inv);
    o.y = pk2(rbf(v[2] - m) * inv, rbf(v[3] - m) * inv);
    o.z = pk2(rbf(v[4] - m) * inv, rbf(v[5] - m) * inv);
    o.w = pk2(rbf(v[6] - m) * inv, rbf(v[7] - m) * inv);
    *(LAS v4u*)(L + L_XN + wv * 1024 + lane * 16) = o;
  }
  VMWAIT(4);
  BARRIER();
  STAMP(2);
  const u32 trq = (i16 >> 2), trp = lane & 3;
  {
    const u32 aoff = L_XN + (lane & 1) * 1024 + (64 * j + 8 * g) * 2;
    const u32 boff = L_WQ + (8 * g + trq) * 256 + wv * 32 + trp * 8;
    v4f d = {0.f, 0.f, 0.f, 0.f};
    d = mfma16(lds128(L, aoff), trfrag(L, boff, 1024), d);
    d = mfma16(lds128(L, aoff + 64), trfrag(L, boff + 32 * 256, 1024), d);
    if (lane < 16) {
      const u32 vo = WS_QP + (h * 8 + j) * 2048 + (16 * wv + lane) * 8;
      const v2u g0 = {__float_as_uint(d[0]), tag}, g1 = {__float_as_uint(d[1]), tag};
      asm volatile("global_store_dwordx2 %0, %1, %3 sc1\n\tglobal_store_dwordx2 %0, %2, %3 offset:1024 sc1" :: "v"(vo), "v"(g0), "v"(g1), "s"(ws) : "memory");
    }
  }
  if (ffn && (wv == 2 || wv == 3)) {
    const u32 c = wv - 2;
    const u32 aoff = L_XN + (lane & 1) * 1024 + (128 * kq + 8 * g) * 2;
    const u32 boff = L_W1 + (8 * g + trq) * 64 + c * 32 + trp * 8;
    v4f d = {0.f, 0.f, 0.f, 0.f};
#pragma unroll
    for (int ks = 0; ks < 4; ks++) d = mfma16(lds128(L, aoff + ks * 64), trfrag(L, boff + ks * 32 * 64, 256), d);
    if (lane < 16) {
      const u32 vo = WS_HP + (kap * 4 + kq) * 512 + (16 * c + lane) * 8;
      const v2u g0 = {__float_as_uint(d[0]), tag}, g1 = {__float_as_uint(d[1]), tag};
      asm volatile("global_store_dwordx2 %0, %1, %3 sc1\n\tglobal_store_dwordx2 %0, %2, %3 offset:256 sc1" :: "v"(vo), "v"(g0), "v"(g1), "s"(ws) : "memory");
    }
  }
  LD16(K[4], vK, kc, 0, KV_NT, "K4");
  LD16(K[5], vK, kc, 1024, KV_NT, "K5");
  LD16(K[6], vK, kc, 2048, KV_NT, "K6");
  LD16(K[7], vK, kc, 3072, KV_NT, "K7");
  LD16(V[0], vV, vc, -4096, KV_NT, "V0");
  LD16(V[1], vV, vc, -3840, KV_NT, "V1");
  LD16(V[2], vV, vc, -3584, KV_NT, "V2");
  LD16(V[3], vV, vc, -3328, KV_NT, "V3");
  LD16(V[4], vV, vc, 0, KV_NT, "V4");
  LD16(V[5], vV, vc, 256, KV_NT, "V5");
  LD16(V[6], vV, vc, 512, KV_NT, "V6");
  LD16(V[7], vV, vc, 768, KV_NT, "V7");
  STAMP(3);
  if (wv == 0) {
    VMWAIT(8);
    const u32 vo = WS_QP + h * 8 * 2048 + b * 1024 + lane * 16 + 4096, vo2 = vo + 8192;
    float q0, q1;
    for (u32 spin = 0;; spin++) {
      v4u p0, p1, p2, p3, p4, p5, p6, p7;
      asm volatile(
          "global_load_dwordx4 %0, %8, %10 offset:-4096 sc1\n\t"
          "global_load_dwordx4 %1, %8, %10 offset:-2048 sc1\n\t"
          "global_load_dwordx4 %2, %8, %10 sc1\n\t"
          "global_load_dwordx4 %3, %8, %10 offset:2048 sc1\n\t"
          "global_load_dwordx4 %4, %9, %10 offset:-4096 sc1\n\t"
          "global_load_dwordx4 %5, %9, %10 offset:-2048 sc1\n\t"
          "global_load_dwordx4 %6, %9, %10 sc1\n\t"
          "global_load_dwordx4 %7, %9, %10 offset:2048 sc1\n\t"
          "s_waitcnt vmcnt(0)"
          : "=&v"(p0), "=&v"(p1), "=&v"(p2), "=&v"(p3), "=&v"(p4), "=&v"(p5), "=&v"(p6), "=&v"(p7)
          : "v"(vo), "v"(vo2), "s"(ws) : "memory");
      const u32 bad = ((p0.y ^ tag) | (p0.w ^ tag) | (p1.y ^ tag) | (p1.w ^ tag)) | ((p2.y ^ tag) | (p2.w ^ tag) | (p3.y ^ tag) | (p3.w ^ tag)) |
                      ((p4.y ^ tag) | (p4.w ^ tag) | (p5.y ^ tag) | (p5.w ^ tag)) | ((p6.y ^ tag) | (p6.w ^ tag) | (p7.y ^ tag) | (p7.w ^ tag));
      q0 = ((__uint_as_float(p0.x) + __uint_as_float(p1.x)) + (__uint_as_float(p2.x) + __uint_as_float(p3.x))) +
           ((__uint_as_float(p4.x) + __uint_as_float(p5.x)) + (__uint_as_float(p6.x) + __uint_as_float(p7.x)));
      q1 = ((__uint_as_float(p0.z) + __uint_as_float(p1.z)) + (__uint_as_float(p2.z) + __uint_as_float(p3.z))) +
           ((__uint_as_float(p4.z) + __uint_as_float(p5.z)) + (__uint_as_float(p6.z) + __uint_as_float(p7.z)));
      if (__all(bad == 0) || spin > SPIN_CAP) break;
      __builtin_amdgcn_s_sleep(2);
    }
    *(LAS u32*)(L + L_QB + lane * 4) = pk2(q0, q1);
    STAMP(4);
  } else if (wv == 1) {
    VMWAIT(8);
    const u32 bb = lane >> 5, f = lane & 31;
    for (u32 ki = 0; ki < nk; ki++) {
      const u32 kp = kap2 + ki;
      const u32 vo = WS_HP + kp * 2048 + bb * 256 + f * 8;
      const u32 vb = (kp * 32 + f) * 2;
      float hs; u32 bias;
      for (u32 spin = 0;; spin++) {
        v2u p0, p1, p2, p3;
        asm volatile(
            "global_load_ushort %4, %6, %8\n\t"
            "global_load_dwordx2 %0, %5, %7 sc1\n\t"
            "global_load_dwordx2 %1, %5, %7 offset:512 sc1\n\t"
            "global_load_dwordx2 %2, %5, %7 offset:1024 sc1\n\t"
            "global_load_dwordx2 %3, %5, %7 offset:1536 sc1\n\t"
            "s_waitcnt vmcnt(0)"
            : "=&v"(p0), "=&v"(p1), "=&v"(p2), "=&v"(p3), "=&v"(bias)
            : "v"(vo), "v"(vb), "s"(ws), "s"(b1) : "memory");
        const u32 bad = (p0.y ^ tag) | (p1.y ^ tag) | (p2.y ^ tag) | (p3.y ^ tag);
        hs = (__uint_as_float(p0.x) + __uint_as_float(p1.x)) + (__uint_as_float(p2.x) + __uint_as_float(p3.x));
        if (__all(bad == 0) || spin > SPIN_CAP) break;
        __builtin_amdgcn_s_sleep(2);
      }
      float hv = rbf(rbf(hs) + bflo(bias));
      hv = hv > 0.f ? hv : 0.f;
      *(LAS u16*)(L + L_HB + bb * 128 + ki * 64 + f * 2) = (u16)pk2(hv * hv, 0.f);
    }
  }
  BARRIER();
  STAMP(5);
  float t0, t1;
  {
    const v4u qc = lds128(L, L_QB + (4 * g + (lane & 3)) * 16);
    v4u A[4];
#pragma unroll
    for (int a = 0; a < 4; a++) {
      const bool on = (i16 >> 2) == (u32)a;
      A[a] = v4u{on ? qc.x : 0u, on ? qc.y : 0u, on ? qc.z : 0u, on ? qc.w : 0u};
    }
    asm volatile("s_waitcnt vmcnt(8) ; GUARD K0 K1 K2 K3 K4 K5 K6 K7" : "+v"(K[0]), "+v"(K[1]), "+v"(K[2]), "+v"(K[3]), "+v"(K[4]), "+v"(K[5]), "+v"(K[6]), "+v"(K[7]) :: "memory");
    v4f d0 = {0.f, 0.f, 0.f, 0.f}, d1 = {0.f, 0.f, 0.f, 0.f};
#pragma unroll
    for (int a = 0; a < 4; a++) { d0 = mfma16(A[a], K[a], d0); d1 = mfma16(A[a], K[4 + a], d1); }
    const bool s1 = lane & 1, s2 = lane & 2;
    float u0 = s2 ? (s1 ? d0[3] : d0[2]) : (s1 ? d0[1] : d0[0]);
    float u1 = s2 ? (s1 ? d1[3] : d1[2]) : (s1 ? d1[1] : d1[0]);
    u0 += dppf<XOR1>(u0); u1 += dppf<XOR1>(u1);
    u0 += dppf<XOR2>(u0); u1 += dppf<XOR2>(u1);
    constexpr float SC = 0.08838834764831845f, L2E = 1.4426950408889634f;
    t0 = rbf(rbf(u0) * SC) * L2E;
    t1 = rbf(rbf(u1) * SC) * L2E;
  }
  {
    float m = fmaxf(t0, t1);
    m = fmaxf(m, dppf<ROR4>(m)); m = fmaxf(m, dppf<ROR8>(m));
    m = x16max(m);
    if (lane == 0) *(LAS float*)(L + L_MX + wv * 4) = m;
  }
  BARRIER();
  STAMP(6);
  {
    const u32 vo = (r * 128 + 16 * wv + (lane >> 3)) * 1024 + xcd * 128 + (lane & 7) * 16;
    DMA16(vo, wo, ldsb + L_WO + wv * 2048, W_NT);
    const u32 vo2 = vo + 8 * 1024;
    DMA16(vo2, wo, ldsb + L_WO + wv * 2048 + 1024, W_NT);
    const u32 lw2 = __builtin_amdgcn_readfirstlane(ldsb + L_W2 + wv * 512 * nk);
    const u32 vw = (kap2 * 32 + wv * 4 * nk + (lane >> 3)) * 1024 + xcd * 128 + (lane & 7) * 16;
    asm volatile("" :: "s"(lw2));
    if (lane < 32 * nk) {
      DMA16(vw, w2, lw2, W_NT);
    }
  }
  float M;
  v4u AV[8];
  {
    const v4f ma = *(const LAS v4f*)(L + L_MX), mb = *(const LAS v4f*)(L + L_MX + 16);
    M = fmaxf(fmaxf(fmaxf(ma[0], ma[1]), fmaxf(ma[2], ma[3])), fmaxf(fmaxf(mb[0], mb[1]), fmaxf(mb[2], mb[3])));
    const float p0 = __builtin_amdgcn_exp2f(t0 - M), p1 = __builtin_amdgcn_exp2f(t1 - M);
    float ls = p0 + p1;
    ls += dppf<ROR4>(ls); ls += dppf<ROR8>(ls);
    ls = x16sum(ls);
    if (lane == 0) *(LAS float*)(L + L_SM + wv * 4) = ls;
    const u32 pp0 = pk2(p0, p0), pp1 = pk2(p1, p1);
    const u32 hm = (lane & 1) ? 0xffff0000u : 0x0000ffffu, kd = (lane & 7) >> 1;
    const u32 mk0 = kd == 0 ? hm : 0u, mk1 = kd == 1 ? hm : 0u, mk2 = kd == 2 ? hm : 0u, mk3 = kd == 3 ? hm : 0u;
#define AVB(t, pp, c) { const u32 z = dppu<BC0 + c>(pp); AV[t] = v4u{z & mk0, z & mk1, z & mk2, z & mk3}; }
    AVB(0, pp0, 0) AVB(1, pp0, 4) AVB(2, pp0, 8) AVB(3, pp0, 12) AVB(4, pp1, 0) AVB(5, pp1, 4) AVB(6, pp1, 8) AVB(7, pp1, 12)
#undef AVB
    asm volatile("" : "+v"(AV[0]), "+v"(AV[1]), "+v"(AV[2]), "+v"(AV[3]), "+v"(AV[4]), "+v"(AV[5]), "+v"(AV[6]), "+v"(AV[7]));
  }
  {
    v4f d = {0.f, 0.f, 0.f, 0.f};
#define PVS(t, n) asm volatile("s_waitcnt vmcnt(" #n ") ; GUARD V" #t : "+v"(V[t]) :: "memory"); d = mfma16(AV[t], V[t], d); __builtin_amdgcn_sched_barrier(0);
    PVS(0, 10) PVS(1, 9) PVS(2, 8) PVS(3, 7) PVS(4, 6) PVS(5, 5) PVS(6, 4) PVS(7, 3)
#undef PVS
    if (lane < 32) *(LAS v4f*)(L + L_RED + wv * 512 + (8 * i16 + 4 * g) * 4) = d;
  }
  BARRIER();
  STAMP(7);
  v4u P0, P1, P2, P3, P4, P5, P6, P7, PM;
  const u32 voP = WS_OP + r * 8 * 1152 + lane * 16 + 4032, voM = WS_OP + (r * 8 + (lane & 7)) * 1152 + 1024;
  if (wv == 0) {
    float o0 = 0.f, o1 = 0.f;
#pragma unroll
    for (int k = 0; k < 8; k++) { const v2f v = *(const LAS v2f*)(L + L_RED + k * 512 + lane * 8); o0 += v.x; o1 += v.y; }
    const u32 vo = WS_OP + (h * 8 + j) * 1152 + lane * 16;
    const v4u gq = {__float_as_uint(o0), tag, __float_as_uint(o1), tag};
    asm volatile("global_store_dwordx4 %0, %1, %2 sc1\n\ts_nop 1" :: "v"(vo), "v"(gq), "s"(ws) : "memory");
    VMWAIT(1);
  } else if (wv == 1) {
    const v4f sa = *(const LAS v4f*)(L + L_SM), sb = *(const LAS v4f*)(L + L_SM + 16);
    const float ls = (((sa[0] + sa[1]) + (sa[2] + sa[3])) + ((sb[0] + sb[1]) + (sb[2] + sb[3])));
    if (lane == 0) {
      const u32 vo = WS_OP + (h * 8 + j) * 1152 + 1024;
      const v4u gq = {__float_as_uint(M), tag, __float_as_uint(ls), tag};
      asm volatile("global_store_dwordx4 %0, %1, %2 sc1\n\ts_nop 1" :: "v"(vo), "v"(gq), "s"(ws) : "memory");
    }
    VMWAIT(1);
  } else if (wv == 2) {
    __builtin_amdgcn_s_sleep(DLY1);
    asm volatile(
        "global_load_dwordx4 %8, %10, %11 sc1 ; LD PM\n\t"
        "global_load_dwordx4 %0, %9, %11 offset:-4032 sc1 ; LD P0\n\t"
        "global_load_dwordx4 %1, %9, %11 offset:-2880 sc1 ; LD P1\n\t"
        "global_load_dwordx4 %2, %9, %11 offset:-1728 sc1 ; LD P2\n\t"
        "global_load_dwordx4 %3, %9, %11 offset:-576 sc1 ; LD P3\n\t"
        "global_load_dwordx4 %4, %9, %11 offset:576 sc1 ; LD P4\n\t"
        "global_load_dwordx4 %5, %9, %11 offset:1728 sc1 ; LD P5\n\t"
        "global_load_dwordx4 %6, %9, %11 offset:2880 sc1 ; LD P6\n\t"
        "global_load_dwordx4 %7, %9, %11 offset:4032 sc1 ; LD P7\n\t"
        "s_waitcnt vmcnt(9)"
        : "=&v"(P0), "=&v"(P1), "=&v"(P2), "=&v"(P3), "=&v"(P4), "=&v"(P5), "=&v"(P6), "=&v"(P7), "=&v"(PM)
        : "v"(voP), "v"(voM), "s"(ws) : "memory");
  } else {
    VMWAIT(0);
  }
  BARRIER();
  STAMP(8);
  v4u BW[4];
  v4f dg = {0.f, 0.f, 0.f, 0.f};
  if (wv >= 4) {
    const u32 t = wv - 4;
    const u32 boff = (8 * g + trq) * 128 + t * 32 + trp * 8;
#pragma unroll
    for (int ks = 0; ks < 4; ks++) BW[ks] = trfrag(L, L_WO + boff + ks * 4096, 512);
    for (u32 ki = 0; ki < nk; ki++)
      dg = mfma16(lds128(L, L_HB + (lane & 1) * 128 + ki * 64 + g * 16), trfrag(L, L_W2 + boff + ki * 4096, 512), dg);
  } else if (wv == 2) {
    asm volatile("s_waitcnt vmcnt(0) ; GUARD P0 P1 P2 P3 P4 P5 P6 P7 PM" : "+v"(P0), "+v"(P1), "+v"(P2), "+v"(P3), "+v"(P4), "+v"(P5), "+v"(P6), "+v"(P7), "+v"(PM) :: "memory");
    v4u p0 = P0, p1 = P1, p2 = P2, p3 = P3, p4 = P4, p5 = P5, p6 = P6, p7 = P7, pm = PM;
    for (u32 spin = 0;; spin++) {
      const u32 bad = ((p0.y ^ tag) | (p0.w ^ tag) | (p1.y ^ tag) | (p1.w ^ tag)) | ((p2.y ^ tag) | (p2.w ^ tag) | (p3.y ^ tag) | (p3.w ^ tag)) |
                      ((p4.y ^ tag) | (p4.w ^ tag) | (p5.y ^ tag) | (p5.w ^ tag)) | ((p6.y ^ tag) | (p6.w ^ tag) | (p7.y ^ tag) | (p7.w ^ tag)) |
                      ((pm.y ^ tag) | (pm.w ^ tag));
      if (__all(bad == 0) || spin > SPIN_CAP) break;
      asm volatile(
          "global_load_dwordx4 %8, %10, %11 sc1\n\t"
          "global_load_dwordx4 %0, %9, %11 offset:-4032 sc1\n\t"
          "global_load_dwordx4 %1, %9, %11 offset:-2880 sc1\n\t"
          "global_load_dwordx4 %2, %9, %11 offset:-1728 sc1\n\t"
          "global_load_dwordx4 %3, %9, %11 offset:-576 sc1\n\t"
          "global_load_dwordx4 %4, %9, %11 offset:576 sc1\n\t"
          "global_load_dwordx4 %5, %9, %11 offset:1728 sc1\n\t"
          "global_load_dwordx4 %6, %9, %11 offset:2880 sc1\n\t"
          "global_load_dwordx4 %7, %9, %11 offset:4032 sc1\n\t"
          "s_waitcnt vmcnt(0)"
          : "=&v"(p0), "=&v"(p1), "=&v"(p2), "=&v"(p3), "=&v"(p4), "=&v"(p5), "=&v"(p6), "=&v"(p7), "=&v"(pm)
          : "v"(voP), "v"(voM), "s"(ws) : "memory");
    }
    STAMP(9);
    const float mp = __uint_as_float(pm.x), lp = __uint_as_float(pm.z);
    float Mb = fmaxf(mp, dppf<XOR1>(mp)); Mb = fmaxf(Mb, dppf<XOR2>(Mb));
    const float e = __builtin_amdgcn_exp2f(mp - Mb);
    float Lb = e * lp; Lb += dppf<XOR1>(Lb); Lb += dppf<XOR2>(Lb);
    const float f = e * __builtin_amdgcn_rcpf(Lb);
    const float f0 = dppf<BC0 + 0>(f), f1 = dppf<BC0 + 1>(f), f2 = dppf<BC0 + 2>(f), f3 = dppf<BC0 + 3>(f);
    const float f4 = dppf<BC0 + 4>(f), f5 = dppf<BC0 + 5>(f), f6 = dppf<BC0 + 6>(f), f7 = dppf<BC0 + 7>(f);
    const float a0 = fmaf(f0, __uint_as_float(p0.x), f1 * __uint_as_float(p1.x)) + fmaf(f2, __uint_as_float(p2.x), f3 * __uint_as_float(p3.x));
    const float a1 = fmaf(f0, __uint_as_float(p0.z), f1 * __uint_as_float(p1.z)) + fmaf(f2, __uint_as_float(p2.z), f3 * __uint_as_float(p3.z));
    const float c0 = fmaf(f4, __uint_as_float(p4.x), f5 * __uint_as_float(p5.x)) + fmaf(f6, __uint_as_float(p6.x), f7 * __uint_as_float(p7.x));
    const float c1 = fmaf(f4, __uint_as_float(p4.z), f5 * __uint_as_float(p5.z)) + fmaf(f6, __uint_as_float(p6.z), f7 * __uint_as_float(p7.z));
    *(LAS u32*)(L + L_ON + lane * 4) = pk2(a0, a1);
    *(LAS u32*)(L + L_ON + 256 + lane * 4) = pk2(c0, c1);
  }
  BARRIER();
  STAMP(10);
  if (wv >= 4) {
    const u32 t = wv - 4;
    const u32 aoff = L_ON + (lane & 1) * 256 + g * 16;
    v4f d = {0.f, 0.f, 0.f, 0.f};
#pragma unroll
    for (int ks = 0; ks < 4; ks++) d = mfma16(lds128(L, aoff + ks * 64), BW[ks], d);
    auto ss = __builtin_amdgcn_permlane16_swap(__float_as_uint(d[0]), __float_as_uint(d[1]), false, false);
    auto gg = __builtin_amdgcn_permlane16_swap(__float_as_uint(dg[0]), __float_as_uint(dg[1]), false, false);
    if (lane < 32) {
      const u32 vo = WS_SP + ((xcd * 32 + r) * 128 + g * 64 + 16 * t + i16) * 16;
      const v4u gq = {ss[0], tag, gg[0], tag};
      asm volatile("global_store_dwordx4 %0, %1, %2 sc1\n\ts_nop 1" :: "v"(vo), "v"(gq), "s"(ws) : "memory");
    }
    STAMP(11);
  } else if (wv == 0 && r < 16) {
    const u32 bb = r >> 3, col = xcd * 64 + (r & 7) * 8 + (lane & 7);
    const u32 vo = WS_SP + ((xcd * 32 + (lane >> 3)) * 128 + bb * 64 + (r & 7) * 8 + (lane & 7)) * 16;
    const u32 vo1 = vo + 8 * 2048, vo2 = vo + 16 * 2048, vo3 = vo + 24 * 2048;
    const u32 vx = (bb * 512 + col) * 2, vb = col * 2;
    u32 xv, bv;
    asm volatile("global_load_ushort %0, %2, %4 ; LD XV\n\tglobal_load_ushort %1, %3, %5 ; LD BV" : "=&v"(xv), "=&v"(bv) : "v"(vx), "v"(vb), "s"(x), "s"(b2) : "memory");
    __builtin_amdgcn_s_sleep(DLY2);
    float s, gs;
    for (u32 spin = 0;; spin++) {
      v4u p0, p1, p2, p3;
      asm volatile(
          "global_load_dwordx4 %0, %4, %8 sc1\n\t"
          "global_load_dwordx4 %1, %5, %8 sc1\n\t"
          "global_load_dwordx4 %2, %6, %8 sc1\n\t"
          "global_load_dwordx4 %3, %7, %8 sc1\n\t"
          "s_waitcnt vmcnt(0)"
          : "=&v"(p0), "=&v"(p1), "=&v"(p2), "=&v"(p3)
          : "v"(vo), "v"(vo1), "v"(vo2), "v"(vo3), "s"(ws) : "memory");
      const u32 bad = ((p0.y ^ tag) | (p0.w ^ tag) | (p1.y ^ tag) | (p1.w ^ tag)) | ((p2.y ^ tag) | (p2.w ^ tag) | (p3.y ^ tag) | (p3.w ^ tag));
      s = (__uint_as_float(p0.x) + __uint_as_float(p1.x)) + (__uint_as_float(p2.x) + __uint_as_float(p3.x));
      gs = (__uint_as_float(p0.z) + __uint_as_float(p1.z)) + (__uint_as_float(p2.z) + __uint_as_float(p3.z));
      if (__all(bad == 0) || spin > SPIN_CAP) break;
    }
    asm volatile("; GUARD XV BV" : "+v"(xv), "+v"(bv));
    s += dppf<ROR8>(s); gs += dppf<ROR8>(gs);
    s = x16sum(s); gs = x16sum(gs);
    const float gt = rbf(rbf(gs) + bflo(bv));
    const float sg = rbf(gt * __builtin_amdgcn_rcpf(1.f + __builtin_amdgcn_exp2f(gt * -1.4426950408889634f)));
    const float o = bflo(xv) + rbf(rbf(s) * sg);
    if (lane < 8) {
      const u32 vo_ = (bb * 512 + col) * 2;
      u32 ob = pk2(o, o);
      asm volatile("global_store_short %0, %1, %2" :: "v"(vo_), "v"(ob), "s"(out) : "memory");
    }
    if (w == 0 && lane == 0) {
      const u32 z = 0;
      asm volatile("global_store_dword %0, %1, %2 sc1" :: "v"(z), "v"(tag), "s"(ws) : "memory");
    }
    STAMP(12);
    if (w == 0) { u64 now; const u64 until = ts[12] + (ts[PROBE_IDX] - ts[0]);
      do { asm volatile("s_memrealtime %0\n\ts_waitcnt lgkmcnt(0)" : "=s"(now) :: "memory"); } while (now < until); }
  }
}

extern "C" void kernel_launch(void* const* d_in, const int* in_sizes, int n_in, void* d_out, int out_size, void* d_ws, size_t ws_size,
                              hipStream_t stream) {
  fused_decode<<<256, 512, 0, stream>>>((const u16*)d_in[0], (const u16*)d_in[1], (const u16*)d_in[2], (const u16*)d_in[3], (const u16*)d_in[4],
                                        (const u16*)d_in[6], (const u16*)d_in[7], (const u16*)d_in[8], (const u16*)d_in[9], (u16*)d_out,
                                        (unsigned char*)d_ws);
}
```

```cpp
#include <hip/hip_runtime.h>
#include <stdint.h>

typedef unsigned u32;
typedef unsigned long long u64;
typedef unsigned short u16;
typedef u32 v4u __attribute__((ext_vector_type(4)));
typedef u32 v2u __attribute__((ext_vector_type(2)));
typedef float v4f __attribute__((ext_vector_type(4)));
typedef float v2f __attribute__((ext_vector_type(2)));
typedef short s8v __attribute__((ext_vector_type(8)));
typedef short s4v __attribute__((ext_vector_type(4)));
typedef __bf16 bf2v __attribute__((ext_vector_type(2)));
#define LAS __attribute__((address_space(3)))
#define CAS __attribute__((address_space(4)))

#define KV_NT " nt"
#define W_NT " nt"
#define DLY1 6
#define DLY2 6
#define SPIN_CAP 200000

constexpr u32 WS_QP = 4096;
constexpr u32 WS_HP = WS_QP + 32 * 8 * 2 * 1024;
constexpr u32 WS_OP = WS_HP + 43 * 4 * 512 + 2048;
constexpr u32 WS_SP = WS_OP + 256 * 1152 + 4096;
static_assert(WS_HP % 128 == 0 && WS_OP % 128 == 0 && WS_SP % 128 == 0, "line aligned");
constexpr u32 L_WQ = 0;
constexpr u32 L_W1 = 16384;
constexpr u32 L_WO = 24576;
constexpr u32 L_W2 = 40960;
constexpr u32 L_XN = 49152;
constexpr u32 L_QB = 51200;
constexpr u32 L_HB = 51456;
constexpr u32 L_ON = 51712;
constexpr u32 L_MX = 52224;
constexpr u32 L_SM = 52256;
constexpr u32 L_RED = 52352;
#define LDSB 86016
constexpr u32 L_BYTES = LDSB;

__device__ __forceinline__ v4f mfma16(v4u a, v4u b, v4f c) {
  return __builtin_amdgcn_mfma_f32_16x16x32_bf16(__builtin_bit_cast(s8v, a), __builtin_bit_cast(s8v, b), c, 0, 0, 0);
}
__device__ __forceinline__ u32 pk2(float lo, float hi) { bf2v z = {(__bf16)lo, (__bf16)hi}; return __builtin_bit_cast(u32, z); }
__device__ __forceinline__ float rbf(float x) { return __uint_as_float(pk2(x, x) & 0xffff0000u); }
__device__ __forceinline__ float bflo(u32 v) { return __uint_as_float(v << 16); }
__device__ __forceinline__ float bfhi(u32 v) { return __uint_as_float(v & 0xffff0000u); }
template <int CTRL> __device__ __forceinline__ float dppf(float x) {
  return __builtin_bit_cast(float, __builtin_amdgcn_mov_dpp(__builtin_bit_cast(int, x), CTRL, 0xf, 0xf, true));
}
template <int CTRL> __device__ __forceinline__ u32 dppu(u32 x) { return (u32)__builtin_amdgcn_mov_dpp((int)x, CTRL, 0xf, 0xf, true); }
constexpr int XOR1 = 0xB1, XOR2 = 0x4E, HMIR = 0x141, MIR = 0x140, ROR4 = 0x124, ROR8 = 0x128, BC0 = 0x150;
__device__ __forceinline__ float x16sum(float x) {
  auto s = __builtin_amdgcn_permlane16_swap(__float_as_uint(x), __float_as_uint(x), false, false);
  x = __uint_as_float(s[0]) + __uint_as_float(s[1]);
  auto t = __builtin_amdgcn_permlane32_swap(__float_as_uint(x), __float_as_uint(x), false, false);
  return __uint_as_float(t[0]) + __uint_as_float(t[1]);
}
__device__ __forceinline__ float x16max(float x) {
  auto s = __builtin_amdgcn_permlane16_swap(__float_as_uint(x), __float_as_uint(x), false, false);
  x = fmaxf(__uint_as_float(s[0]), __uint_as_float(s[1]));
  auto t = __builtin_amdgcn_permlane32_swap(__float_as_uint(x), __float_as_uint(x), false, false);
  return fmaxf(__uint_as_float(t[0]), __uint_as_float(t[1]));
}
__device__ __forceinline__ float wave_sum(float x) {
  x += dppf<XOR1>(x); x += dppf<XOR2>(x); x += dppf<HMIR>(x); x += dppf<MIR>(x);
  return x16sum(x);
}
#define BARRIER() asm volatile("s_waitcnt lgkmcnt(0)\n\ts_barrier" ::: "memory")
#define VMWAIT(n) asm volatile("s_waitcnt vmcnt(" #n ")" ::: "memory")

#define DMA16(voff, base, lds, POL) \
  asm volatile("s_mov_b32 m0, %2\n\ts_nop 0\n\tglobal_load_lds_dwordx4 %0, %1" POL :: "v"(voff), "s"(base), "s"(lds) : "memory")
#define LD16(dst, voff, base, imm, POL, NAME) \
  asm volatile("global_load_dwordx4 %0, %1, %2 offset:" #imm POL " ; LD " NAME : "=v"(dst) : "v"(voff), "s"(base) : "memory")

__device__ __forceinline__ v4u lds128(const LAS unsigned char* L, u32 off) { return *(const LAS v4u*)(L + off); }
__device__ __forceinline__ v4u trfrag(const LAS unsigned char* L, u32 off, u32 step4) {
  s4v lo = __builtin_amdgcn_ds_read_tr16_b64_v4i16((LAS s4v*)(L + off));
  s4v hi = __builtin_amdgcn_ds_read_tr16_b64_v4i16((LAS s4v*)(L + off + step4));
  v2u a = __builtin_bit_cast(v2u, lo), b = __builtin_bit_cast(v2u, hi);
  return v4u{a.x, a.y, b.x, b.y};
}

#define STAMP(i) do {} while (0)

__global__ void __launch_bounds__(512, 2)
fused_decode(const u16* __restrict__ x, const u16* __restrict__ kc, const u16* __restrict__ vc, const u16* __restrict__ wq,
             const u16* __restrict__ wo, const u16* __restrict__ w1, const u16* __restrict__ b1, const u16* __restrict__ w2,
             const u16* __restrict__ b2, u16* __restrict__ out, unsigned char* __restrict__ ws) {
  __shared__ __attribute__((aligned(16))) unsigned char smem[L_BYTES];
  LAS unsigned char* L = (LAS unsigned char*)smem;
  const u32 ldsb = (u32)(size_t)smem;
  STAMP(0);
  asm volatile("" :: "s"(x), "s"(kc), "s"(vc), "s"(wq), "s"(wo), "s"(w1), "s"(b1), "s"(w2), "s"(b2), "s"(out), "s"(ws));
  const u32 tid = threadIdx.x, lane = tid & 63;
  const u32 wv = __builtin_amdgcn_readfirstlane(tid >> 6);
  const u32 w = blockIdx.x, xcd = w & 7, r = w >> 3;
  const u32 h = 4 * xcd + (r >> 3), j = r & 7, b = j >> 2, sp = j & 3;
  const u32 g = lane >> 4, i16 = lane & 15;
  const u32 cmb = (r >> 1) * 8 + xcd, kap = 2 * (cmb >> 2) + (r & 1), kq = cmb & 3;
  const bool ffn = (r < 22) && (kap < 43);
  const u32 nk = r < 21 ? 1 : 2, kap2 = r < 21 ? r : 21 + 2 * (r - 21);
  const u32 tag = *(const CAS u32*)ws + 1u;

  v4u xr;
  if (wv < 2) { const u32 vo = wv * 1024 + lane * 16; LD16(xr, vo, x, 0, "", "X"); }
  {
    const u32 vo = (64 * j + 8 * wv + g) * 8192 + h * 256 + i16 * 16;
    DMA16(vo, wq, ldsb + L_WQ + wv * 2048, W_NT);
    const u32 vo2 = vo + 4 * 8192;
    DMA16(vo2, wq, ldsb + L_WQ + wv * 2048 + 1024, W_NT);
  }
  if (ffn) {
    const u32 vo = (128 * kq + 16 * wv + (lane >> 2)) * 2752 + kap * 64 + (lane & 3) * 16;
    DMA16(vo, w1, ldsb + L_W1 + wv * 1024, W_NT);
  }
  const u32 kvbase = ((b * 32 + h) * 4096 + 3072 + sp * 256 + wv * 32) * 256 + 4096;
  const u32 vK = kvbase + ((lane >> 2) & 3) * 256 + (4 * g + (lane & 3)) * 16;
  const u32 vV = kvbase + g * 1024 + i16 * 16;
  v4u K[8], V[8];
  LD16(K[0], vK, kc, -4096, KV_NT, "K0");
  LD16(K[1], vK, kc, -3072, KV_NT, "K1");
  LD16(K[2], vK, kc, -2048, KV_NT, "K2");
  LD16(K[3], vK, kc, -1024, KV_NT, "K3");
  STAMP(1);
  if (wv < 2) {
    asm volatile("s_waitcnt vmcnt(6) ; GUARD X" : "+v"(xr) :: "memory");
    float v[8] = {bflo(xr.x), bfhi(xr.x), bflo(xr.y), bfhi(xr.y), bflo(xr.z), bfhi(xr.z), bflo(xr.w), bfhi(xr.w)};
    float s = ((v[0] + v[1]) + (v[2] + v[3])) + ((v[4] + v[5]) + (v[6] + v[7]));
    const float mean = wave_sum(s) * (1.f / 512.f);
    float q = 0.f;
#pragma unroll
    for (int k = 0; k < 8; k++) { const float d = v[k] - mean; q = fmaf(d, d, q); }
    const float var = wave_sum(q) * (1.f / 512.f);
    const float m = rbf(mean);
    const float sd = rbf(__builtin_amdgcn_sqrtf(rbf(rbf(var) + 1e-5f)));
    const float inv = __builtin_amdgcn_rcpf(sd);
    v4u o;
    o.x = pk2(rbf(v[0] - m) * inv, rbf(v[1] - m) * inv);
    o.y = pk2(rbf(v[2] - m) * inv, rbf(v[3] - m) * inv);
    o.z = pk2(rbf(v[4] - m) * inv, rbf(v[5] - m) * inv);
    o.w = pk2(rbf(v[6] - m) * inv, rbf(v[7] - m) * inv);
    *(LAS v4u*)(L + L_XN + wv * 1024 + lane * 16) = o;
  }
  VMWAIT(4);
  BARRIER();
  STAMP(2);
  const u32 trq = (i16 >> 2), trp = lane & 3;
  {
    const u32 aoff = L_XN + (lane & 1) * 1024 + (64 * j + 8 * g) * 2;
    const u32 boff = L_WQ + (8 * g + trq) * 256 + wv * 32 + trp * 8;
    v4f d = {0.f, 0.f, 0.f, 0.f};
    d = mfma16(lds128(L, aoff), trfrag(L, boff, 1024), d);
    d = mfma16(lds128(L, aoff + 64), trfrag(L, boff + 32 * 256, 1024), d);
    if (lane < 16) {
      const u32 vo = WS_QP + (h * 8 + j) * 2048 + (16 * wv + lane) * 8;
      const v2u g0 = {__float_as_uint(d[0]), tag}, g1 = {__float_as_uint(d[1]), tag};
      asm volatile("global_store_dwordx2 %0, %1, %3 sc1\n\tglobal_store_dwordx2 %0, %2, %3 offset:1024 sc1" :: "v"(vo), "v"(g0), "v"(g1), "s"(ws) : "memory");
    }
  }
  if (ffn && (wv == 2 || wv == 3)) {
    const u32 c = wv - 2;
    const u32 aoff = L_XN + (lane & 1) * 1024 + (128 * kq + 8 * g) * 2;
    const u32 boff = L_W1 + (8 * g + trq) * 64 + c * 32 + trp * 8;
    v4f d = {0.f, 0.f, 0.f, 0.f};
#pragma unroll
    for (int ks = 0; ks < 4; ks++) d = mfma16(lds128(L, aoff + ks * 64), trfrag(L, boff + ks * 32 * 64, 256), d);
    if (lane < 16) {
      const u32 vo = WS_HP + (kap * 4 + kq) * 512 + (16 * c + lane) * 8;
      const v2u g0 = {__float_as_uint(d[0]), tag}, g1 = {__float_as_uint(d[1]), tag};
      asm volatile("global_store_dwordx2 %0, %1, %3 sc1\n\tglobal_store_dwordx2 %0, %2, %3 offset:256 sc1" :: "v"(vo), "v"(g0), "v"(g1), "s"(ws) : "memory");
    }
  }
  LD16(K[4], vK, kc, 0, KV_NT, "K4");
  LD16(K[5], vK, kc, 1024, KV_NT, "K5");
  LD16(K[6], vK, kc, 2048, KV_NT, "K6");
  LD16(K[7], vK, kc, 3072, KV_NT, "K7");
  LD16(V[0], vV, vc, -4096, KV_NT, "V0");
  LD16(V[1], vV, vc, -3840, KV_NT, "V1");
  LD16(V[2], vV, vc, -3584, KV_NT, "V2");
  LD16(V[3], vV, vc, -3328, KV_NT, "V3");
  LD16(V[4], vV, vc, 0, KV_NT, "V4");
  LD16(V[5], vV, vc, 256, KV_NT, "V5");
  LD16(V[6], vV, vc, 512, KV_NT, "V6");
  LD16(V[7], vV, vc, 768, KV_NT, "V7");
  STAMP(3);
  if (wv == 0) {
    VMWAIT(8);
    const u32 vo = WS_QP + h * 8 * 2048 + b * 1024 + lane * 16 + 4096, vo2 = vo + 8192;
    float q0, q1;
    for (u32 spin = 0;; spin++) {
      v4u p0, p1, p2, p3, p4, p5, p6, p7;
      asm volatile(
          "global_load_dwordx4 %0, %8, %10 offset:-4096 sc1\n\t"
          "global_load_dwordx4 %1, %8, %10 offset:-2048 sc1\n\t"
          "global_load_dwordx4 %2, %8, %10 sc1\n\t"
          "global_load_dwordx4 %3, %8, %10 offset:2048 sc1\n\t"
          "global_load_dwordx4 %4, %9, %10 offset:-4096 sc1\n\t"
          "global_load_dwordx4 %5, %9, %10 offset:-2048 sc1\n\t"
          "global_load_dwordx4 %6, %9, %10 sc1\n\t"
          "global_load_dwordx4 %7, %9, %10 offset:2048 sc1\n\t"
          "s_waitcnt vmcnt(0)"
          : "=&v"(p0), "=&v"(p1), "=&v"(p2), "=&v"(p3), "=&v"(p4), "=&v"(p5), "=&v"(p6), "=&v"(p7)
          : "v"(vo), "v"(vo2), "s"(ws) : "memory");
      const u32 bad = ((p0.y ^ tag) | (p0.w ^ tag) | (p1.y ^ tag) | (p1.w ^ tag)) | ((p2.y ^ tag) | (p2.w ^ tag) | (p3.y ^ tag) | (p3.w ^ tag)) |
                      ((p4.y ^ tag) | (p4.w ^ tag) | (p5.y ^ tag) | (p5.w ^ tag)) | ((p6.y ^ tag) | (p6.w ^ tag) | (p7.y ^ tag) | (p7.w ^ tag));
      q0 = ((__uint_as_float(p0.x) + __uint_as_float(p1.x)) + (__uint_as_float(p2.x) + __uint_as_float(p3.x))) +
           ((__uint_as_float(p4.x) + __uint_as_float(p5.x)) + (__uint_as_float(p6.x) + __uint_as_float(p7.x)));
      q1 = ((__uint_as_float(p0.z) + __uint_as_float(p1.z)) + (__uint_as_float(p2.z) + __uint_as_float(p3.z))) +
           ((__uint_as_float(p4.z) + __uint_as_float(p5.z)) + (__uint_as_float(p6.z) + __uint_as_float(p7.z)));
      if (__all(bad == 0) || spin > SPIN_CAP) break;
      __builtin_amdgcn_s_sleep(2);
    }
    *(LAS u32*)(L + L_QB + lane * 4) = pk2(q0, q1);
    STAMP(4);
  } else if (wv == 1) {
    VMWAIT(8);
    const u32 bb = lane >> 5, f = lane & 31;
    for (u32 ki = 0; ki < nk; ki++) {
      const u32 kp = kap2 + ki;
      const u32 vo = WS_HP + kp * 2048 + bb * 256 + f * 8;
      const u32 vb = (kp * 32 + f) * 2;
      float hs; u32 bias;
      for (u32 spin = 0;; spin++) {
        v2u p0, p1, p2, p3;
        asm volatile(
            "global_load_ushort %4, %6, %8\n\t"
            "global_load_dwordx2 %0, %5, %7 sc1\n\t"
            "global_load_dwordx2 %1, %5, %7 offset:512 sc1\n\t"
            "global_load_dwordx2 %2, %5, %7 offset:1024 sc1\n\t"
            "global_load_dwordx2 %3, %5, %7 offset:1536 sc1\n\t"
            "s_waitcnt vmcnt(0)"
            : "=&v"(p0), "=&v"(p1), "=&v"(p2), "=&v"(p3), "=&v"(bias)
            : "v"(vo), "v"(vb), "s"(ws), "s"(b1) : "memory");
        const u32 bad = (p0.y ^ tag) | (p1.y ^ tag) | (p2.y ^ tag) | (p3.y ^ tag);
        hs = (__uint_as_float(p0.x) + __uint_as_float(p1.x)) + (__uint_as_float(p2.x) + __uint_as_float(p3.x));
        if (__all(bad == 0) || spin > SPIN_CAP) break;
        __builtin_amdgcn_s_sleep(2);
      }
      float hv = rbf(rbf(hs) + bflo(bias));
      hv = hv > 0.f ? hv : 0.f;
      *(LAS u16*)(L + L_HB + bb * 128 + ki * 64 + f * 2) = (u16)pk2(hv * hv, 0.f);
    }
  }
  BARRIER();
  STAMP(5);
  float t0, t1;
  {
    const v4u qc = lds128(L, L_QB + (4 * g + (lane & 3)) * 16);
    v4u A[4];
#pragma unroll
    for (int a = 0; a < 4; a++) {
      const bool on = (i16 >> 2) == (u32)a;
      A[a] = v4u{on ? qc.x : 0u, on ? qc.y : 0u, on ? qc.z : 0u, on ? qc.w : 0u};
    }
    asm volatile("s_waitcnt vmcnt(8) ; GUARD K0 K1 K2 K3 K4 K5 K6 K7" : "+v"(K[0]), "+v"(K[1]), "+v"(K[2]), "+v"(K[3]), "+v"(K[4]), "+v"(K[5]), "+v"(K[6]), "+v"(K[7]) :: "memory");
    v4f d0 = {0.f, 0.f, 0.f, 0.f}, d1 = {0.f, 0.f, 0.f, 0.f};
#pragma unroll
    for (int a = 0; a < 4; a++) { d0 = mfma16(A[a], K[a], d0); d1 = mfma16(A[a], K[4 + a], d1); }
    const bool s1 = lane & 1, s2 = lane & 2;
    float u0 = s2 ? (s1 ? d0[3] : d0[2]) : (s1 ? d0[1] : d0[0]);
    float u1 = s2 ? (s1 ? d1[3] : d1[2]) : (s1 ? d1[1] : d1[0]);
    u0 += dppf<XOR1>(u0); u1 += dppf<XOR1>(u1);
    u0 += dppf<XOR2>(u0); u1 += dppf<XOR2>(u1);
    constexpr float SC = 0.08838834764831845f, L2E = 1.4426950408889634f;
    t0 = rbf(rbf(u0) * SC) * L2E;
    t1 = rbf(rbf(u1) * SC) * L2E;
  }
  {
    float m = fmaxf(t0, t1);
    m = fmaxf(m, dppf<ROR4>(m)); m = fmaxf(m, dppf<ROR8>(m));
    m = x16max(m);
    if (lane == 0) *(LAS float*)(L + L_MX + wv * 4) = m;
  }
  BARRIER();
  STAMP(6);
  {
    const u32 vo = (r * 128 + 16 * wv + (lane >> 3)) * 1024 + xcd * 128 + (lane & 7) * 16;
    DMA16(vo, wo, ldsb + L_WO + wv * 2048, W_NT);
    const u32 vo2 = vo + 8 * 1024;
    DMA16(vo2, wo, ldsb + L_WO + wv * 2048 + 1024, W_NT);
    const u32 lw2 = __builtin_amdgcn_readfirstlane(ldsb + L_W2 + wv * 512 * nk);
    const u32 vw = (kap2 * 32 + wv * 4 * nk + (lane >> 3)) * 1024 + xcd * 128 + (lane & 7) * 16;
    asm volatile("" :: "s"(lw2));
    if (lane < 32 * nk) {
      DMA16(vw, w2, lw2, W_NT);
    }
  }
  float M;
  v4u AV[8];
  {
    const v4f ma = *(const LAS v4f*)(L + L_MX), mb = *(const LAS v4f*)(L + L_MX + 16);
    M = fmaxf(fmaxf(fmaxf(ma[0], ma[1]), fmaxf(ma[2], ma[3])), fmaxf(fmaxf(mb[0], mb[1]), fmaxf(mb[2], mb[3])));
    const float p0 = __builtin_amdgcn_exp2f(t0 - M), p1 = __builtin_amdgcn_exp2f(t1 - M);
    float ls = p0 + p1;
    ls += dppf<ROR4>(ls); ls += dppf<ROR8>(ls);
    ls = x16sum(ls);
    if (lane == 0) *(LAS float*)(L + L_SM + wv * 4) = ls;
    const u32 pp0 = pk2(p0, p0), pp1 = pk2(p1, p1);
    const u32 hm = (lane & 1) ? 0xffff0000u : 0x0000ffffu, kd = (lane & 7) >> 1;
    const u32 mk0 = kd == 0 ? hm : 0u, mk1 = kd == 1 ? hm : 0u, mk2 = kd == 2 ? hm : 0u, mk3 = kd == 3 ? hm : 0u;
#define AVB(t, pp, c) { const u32 z = dppu<BC0 + c>(pp); AV[t] = v4u{z & mk0, z & mk1, z & mk2, z & mk3}; }
    AVB(0, pp0, 0) AVB(1, pp0, 4) AVB(2, pp0, 8) AVB(3, pp0, 12) AVB(4, pp1, 0) AVB(5, pp1, 4) AVB(6, pp1, 8) AVB(7, pp1, 12)
#undef AVB
    asm volatile("" : "+v"(AV[0]), "+v"(AV[1]), "+v"(AV[2]), "+v"(AV[3]), "+v"(AV[4]), "+v"(AV[5]), "+v"(AV[6]), "+v"(AV[7]));
  }
  {
    v4f d = {0.f, 0.f, 0.f, 0.f};
#define PVS(t, n) asm volatile("s_waitcnt vmcnt(" #n ") ; GUARD V" #t : "+v"(V[t]) :: "memory"); d = mfma16(AV[t], V[t], d); __builtin_amdgcn_sched_barrier(0);
    PVS(0, 10) PVS(1, 9) PVS(2, 8) PVS(3, 7) PVS(4, 6) PVS(5, 5) PVS(6, 4) PVS(7, 3)
#undef PVS
    if (lane < 32) *(LAS v4f*)(L + L_RED + wv * 512 + (8 * i16 + 4 * g) * 4) = d;
  }
  BARRIER();
  STAMP(7);
  v4u P0, P1, P2, P3, P4, P5, P6, P7, PM;
  const u32 voP = WS_OP + r * 8 * 1152 + lane * 16 + 4032, voM = WS_OP + (r * 8 + (lane & 7)) * 1152 + 1024;
  if (wv == 0) {
    float o0 = 0.f, o1 = 0.f;
#pragma unroll
    for (int k = 0; k < 8; k++) { const v2f v = *(const LAS v2f*)(L + L_RED + k * 512 + lane * 8); o0 += v.x; o1 += v.y; }
    const u32 vo = WS_OP + (h * 8 + j) * 1152 + lane * 16;
    const v4u gq = {__float_as_uint(o0), tag, __float_as_uint(o1), tag};
    asm volatile("global_store_dwordx4 %0, %1, %2 sc1\n\ts_nop 1" :: "v"(vo), "v"(gq), "s"(ws) : "memory");
    VMWAIT(1);
  } else if (wv == 1) {
    const v4f sa = *(const LAS v4f*)(L + L_SM), sb = *(const LAS v4f*)(L + L_SM + 16);
    const float ls = (((sa[0] + sa[1]) + (sa[2] + sa[3])) + ((sb[0] + sb[1]) + (sb[2] + sb[3])));
    if (lane == 0) {
      const u32 vo = WS_OP + (h * 8 + j) * 1152 + 1024;
      const v4u gq = {__float_as_uint(M), tag, __float_as_uint(ls), tag};
      asm volatile("global_store_dwordx4 %0, %1, %2 sc1\n\ts_nop 1" :: "v"(vo), "v"(gq), "s"(ws) : "memory");
    }
    VMWAIT(1);
  } else if (wv == 2) {
    __builtin_amdgcn_s_sleep(DLY1);
    asm volatile(
        "global_load_dwordx4 %8, %10, %11 sc1 ; LD PM\n\t"
        "global_load_dwordx4 %0, %9, %11 offset:-4032 sc1 ; LD P0\n\t"
        "global_load_dwordx4 %1, %9, %11 offset:-2880 sc1 ; LD P1\n\t"
        "global_load_dwordx4 %2, %9, %11 offset:-1728 sc1 ; LD P2\n\t"
        "global_load_dwordx4 %3, %9, %11 offset:-576 sc1 ; LD P3\n\t"
        "global_load_dwordx4 %4, %9, %11 offset:576 sc1 ; LD P4\n\t"
        "global_load_dwordx4 %5, %9, %11 offset:1728 sc1 ; LD P5\n\t"
        "global_load_dwordx4 %6, %9, %11 offset:2880 sc1 ; LD P6\n\t"
        "global_load_dwordx4 %7, %9, %11 offset:4032 sc1 ; LD P7\n\t"
        "s_waitcnt vmcnt(9)"
        : "=&v"(P0), "=&v"(P1), "=&v"(P2), "=&v"(P3), "=&v"(P4), "=&v"(P5), "=&v"(P6), "=&v"(P7), "=&v"(PM)
        : "v"(voP), "v"(voM), "s"(ws) : "memory");
  } else {
    VMWAIT(0);
  }
  BARRIER();
  STAMP(8);
  v4u BW[4];
  v4f dg = {0.f, 0.f, 0.f, 0.f};
  if (wv >= 4) {
    const u32 t = wv - 4;
    const u32 boff = (8 * g + trq) * 128 + t * 32 + trp * 8;
#pragma unroll
    for (int ks = 0; ks < 4; ks++) BW[ks] = trfrag(L, L_WO + boff + ks * 4096, 512);
    for (u32 ki = 0; ki < nk; ki++)
      dg = mfma16(lds128(L, L_HB + (lane & 1) * 128 + ki * 64 + g * 16), trfrag(L, L_W2 + boff + ki * 4096, 512), dg);
  } else if (wv == 2) {
    asm volatile("s_waitcnt vmcnt(0) ; GUARD P0 P1 P2 P3 P4 P5 P6 P7 PM" : "+v"(P0), "+v"(P1), "+v"(P2), "+v"(P3), "+v"(P4), "+v"(P5), "+v"(P6), "+v"(P7), "+v"(PM) :: "memory");
    v4u p0 = P0, p1 = P1, p2 = P2, p3 = P3, p4 = P4, p5 = P5, p6 = P6, p7 = P7, pm = PM;
    for (u32 spin = 0;; spin++) {
      const u32 bad = ((p0.y ^ tag) | (p0.w ^ tag) | (p1.y ^ tag) | (p1.w ^ tag)) | ((p2.y ^ tag) | (p2.w ^ tag) | (p3.y ^ tag) | (p3.w ^ tag)) |
                      ((p4.y ^ tag) | (p4.w ^ tag) | (p5.y ^ tag) | (p5.w ^ tag)) | ((p6.y ^ tag) | (p6.w ^ tag) | (p7.y ^ tag) | (p7.w ^ tag)) |
                      ((pm.y ^ tag) | (pm.w ^ tag));
      if (__all(bad == 0) || spin > SPIN_CAP) break;
      asm volatile(
          "global_load_dwordx4 %8, %10, %11 sc1\n\t"
          "global_load_dwordx4 %0, %9, %11 offset:-4032 sc1\n\t"
          "global_load_dwordx4 %1, %9, %11 offset:-2880 sc1\n\t"
          "global_load_dwordx4 %2, %9, %11 offset:-1728 sc1\n\t"
          "global_load_dwordx4 %3, %9, %11 offset:-576 sc1\n\t"
          "global_load_dwordx4 %4, %9, %11 offset:576 sc1\n\t"
          "global_load_dwordx4 %5, %9, %11 offset:1728 sc1\n\t"
          "global_load_dwordx4 %6, %9, %11 offset:2880 sc1\n\t"
          "global_load_dwordx4 %7, %9, %11 offset:4032 sc1\n\t"
          "s_waitcnt vmcnt(0)"
          : "=&v"(p0), "=&v"(p1), "=&v"(p2), "=&v"(p3), "=&v"(p4), "=&v"(p5), "=&v"(p6), "=&v"(p7), "=&v"(pm)
          : "v"(voP), "v"(voM), "s"(ws) : "memory");
    }
    STAMP(9);
    const float mp = __uint_as_float(pm.x), lp = __uint_as_float(pm.z);
    float Mb = fmaxf(mp, dppf<XOR1>(mp)); Mb = fmaxf(Mb, dppf<XOR2>(Mb));
    const float e = __builtin_amdgcn_exp2f(mp - Mb);
    float Lb = e * lp; Lb += dppf<XOR1>(Lb); Lb += dppf<XOR2>(Lb);
    const float f = e * __builtin_amdgcn_rcpf(Lb);
    const float f0 = dppf<BC0 + 0>(f), f1 = dppf<BC0 + 1>(f), f2 = dppf<BC0 + 2>(f), f3 = dppf<BC0 + 3>(f);
    const float f4 = dppf<BC0 + 4>(f), f5 = dppf<BC0 + 5>(f), f6 = dppf<BC0 + 6>(f), f7 = dppf<BC0 + 7>(f);
    const float a0 = fmaf(f0, __uint_as_float(p0.x), f1 * __uint_as_float(p1.x)) + fmaf(f2, __uint_as_float(p2.x), f3 * __uint_as_float(p3.x));
    const float a1 = fmaf(f0, __uint_as_float(p0.z), f1 * __uint_as_float(p1.z)) + fmaf(f2, __uint_as_float(p2.z), f3 * __uint_as_float(p3.z));
    const float c0 = fmaf(f4, __uint_as_float(p4.x), f5 * __uint_as_float(p5.x)) + fmaf(f6, __uint_as_float(p6.x), f7 * __uint_as_float(p7.x));
    const float c1 = fmaf(f4, __uint_as_float(p4.z), f5 * __uint_as_float(p5.z)) + fmaf(f6, __uint_as_float(p6.z), f7 * __uint_as_float(p7.z));
    *(LAS u32*)(L + L_ON + lane * 4) = pk2(a0, a1);
    *(LAS u32*)(L + L_ON + 256 + lane * 4) = pk2(c0, c1);
  }
  BARRIER();
  STAMP(10);
  if (wv >= 4) {
    const u32 t = wv - 4;
    const u32 aoff = L_ON + (lane & 1) * 256 + g * 16;
    v4f d = {0.f, 0.f, 0.f, 0.f};
#pragma unroll
    for (int ks = 0; ks < 4; ks++) d = mfma16(lds128(L, aoff + ks * 64), BW[ks], d);
    auto ss = __builtin_amdgcn_permlane16_swap(__float_as_uint(d[0]), __float_as_uint(d[1]), false, false);
    auto gg = __builtin_amdgcn_permlane16_swap(__float_as_uint(dg[0]), __float_as_uint(dg[1]), false, false);
    if (lane < 32) {
      const u32 vo = WS_SP + ((xcd * 32 + r) * 128 + g * 64 + 16 * t + i16) * 16;
      const v4u gq = {ss[0], tag, gg[0], tag};
      asm volatile("global_store_dwordx4 %0, %1, %2 sc1\n\ts_nop 1" :: "v"(vo), "v"(gq), "s"(ws) : "memory");
    }
    STAMP(11);
  } else if (wv == 0 && r < 16) {
    const u32 bb = r >> 3, col = xcd * 64 + (r & 7) * 8 + (lane & 7);
    const u32 vo = WS_SP + ((xcd * 32 + (lane >> 3)) * 128 + bb * 64 + (r & 7) * 8 + (lane & 7)) * 16;
    const u32 vo1 = vo + 8 * 2048, vo2 = vo + 16 * 2048, vo3 = vo + 24 * 2048;
    const u32 vx = (bb * 512 + col) * 2, vb = col * 2;
    u32 xv, bv;
    asm volatile("global_load_ushort %0, %2, %4 ; LD XV\n\tglobal_load_ushort %1, %3, %5 ; LD BV" : "=&v"(xv), "=&v"(bv) : "v"(vx), "v"(vb), "s"(x), "s"(b2) : "memory");
    __builtin_amdgcn_s_sleep(DLY2);
    float s, gs;
    for (u32 spin = 0;; spin++) {
      v4u p0, p1, p2, p3;
      asm volatile(
          "global_load_dwordx4 %0, %4, %8 sc1\n\t"
          "global_load_dwordx4 %1, %5, %8 sc1\n\t"
          "global_load_dwordx4 %2, %6, %8 sc1\n\t"
          "global_load_dwordx4 %3, %7, %8 sc1\n\t"
          "s_waitcnt vmcnt(0)"
          : "=&v"(p0), "=&v"(p1), "=&v"(p2), "=&v"(p3)
          : "v"(vo), "v"(vo1), "v"(vo2), "v"(vo3), "s"(ws) : "memory");
      const u32 bad = ((p0.y ^ tag) | (p0.w ^ tag) | (p1.y ^ tag) | (p1.w ^ tag)) | ((p2.y ^ tag) | (p2.w ^ tag) | (p3.y ^ tag) | (p3.w ^ tag));
      s = (__uint_as_float(p0.x) + __uint_as_float(p1.x)) + (__uint_as_float(p2.x) + __uint_as_float(p3.x));
      gs = (__uint_as_float(p0.z) + __uint_as_float(p1.z)) + (__uint_as_float(p2.z) + __uint_as_float(p3.z));
      if (__all(bad == 0) || spin > SPIN_CAP) break;
    }
    asm volatile("; GUARD XV BV" : "+v"(xv), "+v"(bv));
    s += dppf<ROR8>(s); gs += dppf<ROR8>(gs);
    s = x16sum(s); gs = x16sum(gs);
    const float gt = rbf(rbf(gs) + bflo(bv));
    const float sg = rbf(gt * __builtin_amdgcn_rcpf(1.f + __builtin_amdgcn_exp2f(gt * -1.4426950408889634f)));
    const float o = bflo(xv) + rbf(rbf(s) * sg);
    if (lane < 8) {
      const u32 vo_ = (bb * 512 + col) * 2;
      u32 ob = pk2(o, o);
      asm volatile("global_store_short %0, %1, %2" :: "v"(vo_), "v"(ob), "s"(out) : "memory");
    }
    if (w == 0 && lane == 0) {
      const u32 z = 0;
      asm volatile("global_store_dword %0, %1, %2 sc1" :: "v"(z), "v"(tag), "s"(ws) : "memory");
    }
    STAMP(12);
  }
}

extern "C" void kernel_launch(void* const* d_in, const int* in_sizes, int n_in, void* d_out, int out_size, void* d_ws, size_t ws_size,
                              hipStream_t stream) {
  fused_decode<<<256, 512, 0, stream>>>((const u16*)d_in[0], (const u16*)d_in[1], (const u16*)d_in[2], (const u16*)d_in[3], (const u16*)d_in[4],
                                        (const u16*)d_in[6], (const u16*)d_in[7], (const u16*)d_in[8], (const u16*)d_in[9], (u16*)d_out,
                                        (unsigned char*)d_ws);
}
```

```cpp
#define NTHR 576
#define WBL2 0
#define WBL2_ALL 0
#define WBL2_R 99
#include <hip/hip_runtime.h>
#include <stdint.h>

typedef unsigned u32;
typedef unsigned long long u64;
typedef unsigned short u16;
typedef u32 v4u __attribute__((ext_vector_type(4)));
typedef u32 v2u __attribute__((ext_vector_type(2)));
typedef float v4f __attribute__((ext_vector_type(4)));
typedef float v2f __attribute__((ext_vector_type(2)));
typedef short s8v __attribute__((ext_vector_type(8)));
typedef short s4v __attribute__((ext_vector_type(4)));
typedef __bf16 bf2v __attribute__((ext_vector_type(2)));
#define LAS __attribute__((address_space(3)))
#define CAS __attribute__((address_space(4)))

#define KV_NT " nt"
#define W_NT " nt"
#define DLY1 6
#define DLY2 6
#define SPIN_CAP 200000

constexpr u32 WS_QP = 4096;
constexpr u32 WS_HP = WS_QP + 32 * 8 * 2 * 1024;
constexpr u32 WS_OP = WS_HP + 43 * 4 * 512 + 2048;
constexpr u32 WS_SP = WS_OP + 256 * 1152 + 4096;
static_assert(WS_HP % 128 == 0 && WS_OP % 128 == 0 && WS_SP % 128 == 0, "line aligned");
constexpr u32 L_WQ = 0;
constexpr u32 L_W1 = 16384;
constexpr u32 L_WO = 24576;
constexpr u32 L_W2 = 40960;
constexpr u32 L_XN = 49152;
constexpr u32 L_QB = 51200;
constexpr u32 L_HB = 51456;
constexpr u32 L_ON = 51712;
constexpr u32 L_MX = 52224;
constexpr u32 L_SM = 52256;
constexpr u32 L_RED = 52352;
#define LDSB 86016
constexpr u32 L_BYTES = LDSB;

__device__ __forceinline__ v4f mfma16(v4u a, v4u b, v4f c) {
  return __builtin_amdgcn_mfma_f32_16x16x32_bf16(__builtin_bit_cast(s8v, a), __builtin_bit_cast(s8v, b), c, 0, 0, 0);
}
__device__ __forceinline__ u32 pk2(float lo, float hi) { bf2v z = {(__bf16)lo, (__bf16)hi}; return __builtin_bit_cast(u32, z); }
__device__ __forceinline__ float rbf(float x) { return __uint_as_float(pk2(x, x) & 0xffff0000u); }
__device__ __forceinline__ float bflo(u32 v) { return __uint_as_float(v << 16); }
__device__ __forceinline__ float bfhi(u32 v) { return __uint_as_float(v & 0xffff0000u); }
template <int CTRL> __device__ __forceinline__ float dppf(float x) {
  return __builtin_bit_cast(float, __builtin_amdgcn_mov_dpp(__builtin_bit_cast(int, x), CTRL, 0xf, 0xf, true));
}
template <int CTRL> __device__ __forceinline__ u32 dppu(u32 x) { return (u32)__builtin_amdgcn_mov_dpp((int)x, CTRL, 0xf, 0xf, true); }
constexpr int XOR1 = 0xB1, XOR2 = 0x4E, HMIR = 0x141, MIR = 0x140, ROR4 = 0x124, ROR8 = 0x128, BC0 = 0x150;
__device__ __forceinline__ float x16sum(float x) {
  auto s = __builtin_amdgcn_permlane16_swap(__float_as_uint(x), __float_as_uint(x), false, false);
  x = __uint_as_float(s[0]) + __uint_as_float(s[1]);
  auto t = __builtin_amdgcn_permlane32_swap(__float_as_uint(x), __float_as_uint(x), false, false);
  return __uint_as_float(t[0]) + __uint_as_float(t[1]);
}
__device__ __forceinline__ float x16max(float x) {
  auto s = __builtin_amdgcn_permlane16_swap(__float_as_uint(x), __float_as_uint(x), false, false);
  x = fmaxf(__uint_as_float(s[0]), __uint_as_float(s[1]));
  auto t = __builtin_amdgcn_permlane32_swap(__float_as_uint(x), __float_as_uint(x), false, false);
  return fmaxf(__uint_as_float(t[0]), __uint_as_float(t[1]));
}
__device__ __forceinline__ float wave_sum(float x) {
  x += dppf<XOR1>(x); x += dppf<XOR2>(x); x += dppf<HMIR>(x); x += dppf<MIR>(x);
  return x16sum(x);
}
#define BARRIER() asm volatile("s_waitcnt lgkmcnt(0)\n\ts_barrier" ::: "memory")
#define VMWAIT(n) asm volatile("s_waitcnt vmcnt(" #n ")" ::: "memory")

#define DMA16(voff, base, lds, POL) \
  asm volatile("s_mov_b32 m0, %2\n\ts_nop 0\n\tglobal_load_lds_dwordx4 %0, %1" POL :: "v"(voff), "s"(base), "s"(lds) : "memory")
#define LD16(dst, voff, base, imm, POL, NAME) \
  asm volatile("global_load_dwordx4 %0, %1, %2 offset:" #imm POL " ; LD " NAME : "=v"(dst) : "v"(voff), "s"(base) : "memory")

__device__ __forceinline__ v4u lds128(const LAS unsigned char* L, u32 off) { return *(const LAS v4u*)(L + off); }
__device__ __forceinline__ v4u trfrag(const LAS unsigned char* L, u32 off, u32 step4) {
  s4v lo = __builtin_amdgcn_ds_read_tr16_b64_v4i16((LAS s4v*)(L + off));
  s4v hi = __builtin_amdgcn_ds_read_tr16_b64_v4i16((LAS s4v*)(L + off + step4));
  v2u a = __builtin_bit_cast(v2u, lo), b = __builtin_bit_cast(v2u, hi);
  return v4u{a.x, a.y, b.x, b.y};
}

#define STAMP(i) do {} while (0)

__global__ void __launch_bounds__(NTHR, 2)
fused_decode(const u16* __restrict__ x, const u16* __restrict__ kc, const u16* __restrict__ vc, const u16* __restrict__ wq,
             const u16* __restrict__ wo, const u16* __restrict__ w1, const u16* __restrict__ b1, const u16* __restrict__ w2,
             const u16* __restrict__ b2, u16* __restrict__ out, unsigned char* __restrict__ ws) {
  __shared__ __attribute__((aligned(16))) unsigned char smem[L_BYTES];
  LAS unsigned char* L = (LAS unsigned char*)smem;
  const u32 ldsb = (u32)(size_t)smem;
  STAMP(0);
  asm volatile("" :: "s"(x), "s"(kc), "s"(vc), "s"(wq), "s"(wo), "s"(w1), "s"(b1), "s"(w2), "s"(b2), "s"(out), "s"(ws));
  const u32 tid = threadIdx.x, lane = tid & 63;
  const u32 wv = __builtin_amdgcn_readfirstlane(tid >> 6);
  const u32 w = blockIdx.x, xcd = w & 7, r = w >> 3;
  if (wv == 8) {
    if (WBL2_ALL || r == WBL2_R) {
      u64 now, t00; asm volatile("s_memrealtime %0\n\ts_waitcnt lgkmcnt(0)" : "=s"(t00) :: "memory");
      do { asm volatile("s_memrealtime %0\n\ts_waitcnt lgkmcnt(0)" : "=s"(now) :: "memory"); } while (now < t00 + WBL2);
      asm volatile("buffer_wbl2 sc1\n\ts_waitcnt vmcnt(0)" ::: "memory");
    }
    return;
  }
  const u32 h = 4 * xcd + (r >> 3), j = r & 7, b = j >> 2, sp = j & 3;
  const u32 g = lane >> 4, i16 = lane & 15;
  const u32 cmb = (r >> 1) * 8 + xcd, kap = 2 * (cmb >> 2) + (r & 1), kq = cmb & 3;
  const bool ffn = (r < 22) && (kap < 43);
  const u32 nk = r < 21 ? 1 : 2, kap2 = r < 21 ? r : 21 + 2 * (r - 21);
  const u32 tag = *(const CAS u32*)ws + 1u;

  v4u xr;
  if (wv < 2) { const u32 vo = wv * 1024 + lane * 16; LD16(xr, vo, x, 0, "", "X"); }
  {
    const u32 vo = (64 * j + 8 * wv + g) * 8192 + h * 256 + i16 * 16;
    DMA16(vo, wq, ldsb + L_WQ + wv * 2048, W_NT);
    const u32 vo2 = vo + 4 * 8192;
    DMA16(vo2, wq, ldsb + L_WQ + wv * 2048 + 1024, W_NT);
  }
  if (ffn) {
    const u32 vo = (128 * kq + 16 * wv + (lane >> 2)) * 2752 + kap * 64 + (lane & 3) * 16;
    DMA16(vo, w1, ldsb + L_W1 + wv * 1024, W_NT);
  }
  const u32 kvbase = ((b * 32 + h) * 4096 + 3072 + sp * 256 + wv * 32) * 256 + 4096;
  const u32 vK = kvbase + ((lane >> 2) & 3) * 256 + (4 * g + (lane & 3)) * 16;
  const u32 vV = kvbase + g * 1024 + i16 * 16;
  v4u K[8], V[8];
  LD16(K[0], vK, kc, -4096, KV_NT, "K0");
  LD16(K[1], vK, kc, -3072, KV_NT, "K1");
  LD16(K[2], vK, kc, -2048, KV_NT, "K2");
  LD16(K[3], vK, kc, -1024, KV_NT, "K3");
  STAMP(1);
  if (wv < 2) {
    asm volatile("s_waitcnt vmcnt(6) ; GUARD X" : "+v"(xr) :: "memory");
    float v[8] = {bflo(xr.x), bfhi(xr.x), bflo(xr.y), bfhi(xr.y), bflo(xr.z), bfhi(xr.z), bflo(xr.w), bfhi(xr.w)};
    float s = ((v[0] + v[1]) + (v[2] + v[3])) + ((v[4] + v[5]) + (v[6] + v[7]));
    const float mean = wave_sum(s) * (1.f / 512.f);
    float q = 0.f;
#pragma unroll
    for (int k = 0; k < 8; k++) { const float d = v[k] - mean; q = fmaf(d, d, q); }
    const float var = wave_sum(q) * (1.f / 512.f);
    const float m = rbf(mean);
    const float sd = rbf(__builtin_amdgcn_sqrtf(rbf(rbf(var) + 1e-5f)));
    const float inv = __builtin_amdgcn_rcpf(sd);
    v4u o;
    o.x = pk2(rbf(v[0] - m) * inv, rbf(v[1] - m) * inv);
    o.y = pk2(rbf(v[2] - m) * inv, rbf(v[3] - m) * inv);
    o.z = pk2(rbf(v[4] - m) * inv, rbf(v[5] - m) * inv);
    o.w = pk2(rbf(v[6] - m) * inv, rbf(v[7] - m) * inv);
    *(LAS v4u*)(L + L_XN + wv * 1024 + lane * 16) = o;
  }
  VMWAIT(4);
  BARRIER();
  STAMP(2);
  const u32 trq = (i16 >> 2), trp = lane & 3;
  {
    const u32 aoff = L_XN + (lane & 1) * 1024 + (64 * j + 8 * g) * 2;
    const u32 boff = L_WQ + (8 * g + trq) * 256 + wv * 32 + trp * 8;
    v4f d = {0.f, 0.f, 0.f, 0.f};
    d = mfma16(lds128(L, aoff), trfrag(L, boff, 1024), d);
    d = mfma16(lds128(L, aoff + 64), trfrag(L, boff + 32 * 256, 1024), d);
    if (lane < 16) {
      const u32 vo = WS_QP + (h * 8 + j) * 2048 + (16 * wv + lane) * 8;
      const v2u g0 = {__float_as_uint(d[0]), tag}, g1 = {__float_as_uint(d[1]), tag};
      asm volatile("global_store_dwordx2 %0, %1, %3 sc1\n\tglobal_store_dwordx2 %0, %2, %3 offset:1024 sc1" :: "v"(vo), "v"(g0), "v"(g1), "s"(ws) : "memory");
    }
  }
  if (ffn && (wv == 2 || wv == 3)) {
    const u32 c = wv - 2;
    const u32 aoff = L_XN + (lane & 1) * 1024 + (128 * kq + 8 * g) * 2;
    const u32 boff = L_W1 + (8 * g + trq) * 64 + c * 32 + trp * 8;
    v4f d = {0.f, 0.f, 0.f, 0.f};
#pragma unroll
    for (int ks = 0; ks < 4; ks++) d = mfma16(lds128(L, aoff + ks * 64), trfrag(L, boff + ks * 32 * 64, 256), d);
    if (lane < 16) {
      const u32 vo = WS_HP + (kap * 4 + kq) * 512 + (16 * c + lane) * 8;
      const v2u g0 = {__float_as_uint(d[0]), tag}, g1 = {__float_as_uint(d[1]), tag};
      asm volatile("global_store_dwordx2 %0, %1, %3 sc1\n\tglobal_store_dwordx2 %0, %2, %3 offset:256 sc1" :: "v"(vo), "v"(g0), "v"(g1), "s"(ws) : "memory");
    }
  }
  LD16(K[4], vK, kc, 0, KV_NT, "K4");
  LD16(K[5], vK, kc, 1024, KV_NT, "K5");
  LD16(K[6], vK, kc, 2048, KV_NT, "K6");
  LD16(K[7], vK, kc, 3072, KV_NT, "K7");
  LD16(V[0], vV, vc, -4096, KV_NT, "V0");
  LD16(V[1], vV, vc, -3840, KV_NT, "V1");
  LD16(V[2], vV, vc, -3584, KV_NT, "V2");
  LD16(V[3], vV, vc, -3328, KV_NT, "V3");
  LD16(V[4], vV, vc, 0, KV_NT, "V4");
  LD16(V[5], vV, vc, 256, KV_NT, "V5");
  LD16(V[6], vV, vc, 512, KV_NT, "V6");
  LD16(V[7], vV, vc, 768, KV_NT, "V7");
  STAMP(3);
  if (wv == 0) {
    VMWAIT(8);
    const u32 vo = WS_QP + h * 8 * 2048 + b * 1024 + lane * 16 + 4096, vo2 = vo + 8192;
    float q0, q1;
    for (u32 spin = 0;; spin++) {
      v4u p0, p1, p2, p3, p4, p5, p6, p7;
      asm volatile(
          "global_load_dwordx4 %0, %8, %10 offset:-4096 sc1\n\t"
          "global_load_dwordx4 %1, %8, %10 offset:-2048 sc1\n\t"
          "global_load_dwordx4 %2, %8, %10 sc1\n\t"
          "global_load_dwordx4 %3, %8, %10 offset:2048 sc1\n\t"
          "global_load_dwordx4 %4, %9, %10 offset:-4096 sc1\n\t"
          "global_load_dwordx4 %5, %9, %10 offset:-2048 sc1\n\t"
          "global_load_dwordx4 %6, %9, %10 sc1\n\t"
          "global_load_dwordx4 %7, %9, %10 offset:2048 sc1\n\t"
          "s_waitcnt vmcnt(0)"
          : "=&v"(p0), "=&v"(p1), "=&v"(p2), "=&v"(p3), "=&v"(p4), "=&v"(p5), "=&v"(p6), "=&v"(p7)
          : "v"(vo), "v"(vo2), "s"(ws) : "memory");
      const u32 bad = ((p0.y ^ tag) | (p0.w ^ tag) | (p1.y ^ tag) | (p1.w ^ tag)) | ((p2.y ^ tag) | (p2.w ^ tag) | (p3.y ^ tag) | (p3.w ^ tag)) |
                      ((p4.y ^ tag) | (p4.w ^ tag) | (p5.y ^ tag) | (p5.w ^ tag)) | ((p6.y ^ tag) | (p6.w ^ tag) | (p7.y ^ tag) | (p7.w ^ tag));
      q0 = ((__uint_as_float(p0.x) + __uint_as_float(p1.x)) + (__uint_as_float(p2.x) + __uint_as_float(p3.x))) +
           ((__uint_as_float(p4.x) + __uint_as_float(p5.x)) + (__uint_as_float(p6.x) + __uint_as_float(p7.x)));
      q1 = ((__uint_as_float(p0.z) + __uint_as_float(p1.z)) + (__uint_as_float(p2.z) + __uint_as_float(p3.z))) +
           ((__uint_as_float(p4.z) + __uint_as_float(p5.z)) + (__uint_as_float(p6.z) + __uint_as_float(p7.z)));
      if (__all(bad == 0) || spin > SPIN_CAP) break;
      __builtin_amdgcn_s_sleep(2);
    }
    *(LAS u32*)(L + L_QB + lane * 4) = pk2(q0, q1);
    STAMP(4);
  } else if (wv == 1) {
    VMWAIT(8);
    const u32 bb = lane >> 5, f = lane & 31;
    for (u32 ki = 0; ki < nk; ki++) {
      const u32 kp = kap2 + ki;
      const u32 vo = WS_HP + kp * 2048 + bb * 256 + f * 8;
      const u32 vb = (kp * 32 + f) * 2;
      float hs; u32 bias;
      for (u32 spin = 0;; spin++) {
        v2u p0, p1, p2, p3;
        asm volatile(
            "global_load_ushort %4, %6, %8\n\t"
            "global_load_dwordx2 %0, %5, %7 sc1\n\t"
            "global_load_dwordx2 %1, %5, %7 offset:512 sc1\n\t"
            "global_load_dwordx2 %2, %5, %7 offset:1024 sc1\n\t"
            "global_load_dwordx2 %3, %5, %7 offset:1536 sc1\n\t"
            "s_waitcnt vmcnt(0)"
            : "=&v"(p0), "=&v"(p1), "=&v"(p2), "=&v"(p3), "=&v"(bias)
            : "v"(vo), "v"(vb), "s"(ws), "s"(b1) : "memory");
        const u32 bad = (p0.y ^ tag) | (p1.y ^ tag) | (p2.y ^ tag) | (p3.y ^ tag);
        hs = (__uint_as_float(p0.x) + __uint_as_float(p1.x)) + (__uint_as_float(p2.x) + __uint_as_float(p3.x));
        if (__all(bad == 0) || spin > SPIN_CAP) break;
        __builtin_amdgcn_s_sleep(2);
      }
      float hv = rbf(rbf(hs) + bflo(bias));
      hv = hv > 0.f ? hv : 0.f;
      *(LAS u16*)(L + L_HB + bb * 128 + ki * 64 + f * 2) = (u16)pk2(hv * hv, 0.f);
    }
  }
  BARRIER();
  STAMP(5);
  float t0, t1;
  {
    const v4u qc = lds128(L, L_QB + (4 * g + (lane & 3)) * 16);
    v4u A[4];
#pragma unroll
    for (int a = 0; a < 4; a++) {
      const bool on = (i16 >> 2) == (u32)a;
      A[a] = v4u{on ? qc.x : 0u, on ? qc.y : 0u, on ? qc.z : 0u, on ? qc.w : 0u};
    }
    asm volatile("s_waitcnt vmcnt(8) ; GUARD K0 K1 K2 K3 K4 K5 K6 K7" : "+v"(K[0]), "+v"(K[1]), "+v"(K[2]), "+v"(K[3]), "+v"(K[4]), "+v"(K[5]), "+v"(K[6]), "+v"(K[7]) :: "memory");
    v4f d0 = {0.f, 0.f, 0.f, 0.f}, d1 = {0.f, 0.f, 0.f, 0.f};
#pragma unroll
    for (int a = 0; a < 4; a++) { d0 = mfma16(A[a], K[a], d0); d1 = mfma16(A[a], K[4 + a], d1); }
    const bool s1 = lane & 1, s2 = lane & 2;
    float u0 = s2 ? (s1 ? d0[3] : d0[2]) : (s1 ? d0[1] : d0[0]);
    float u1 = s2 ? (s1 ? d1[3] : d1[2]) : (s1 ? d1[1] : d1[0]);
    u0 += dppf<XOR1>(u0); u1 += dppf<XOR1>(u1);
    u0 += dppf<XOR2>(u0); u1 += dppf<XOR2>(u1);
    constexpr float SC = 0.08838834764831845f, L2E = 1.4426950408889634f;
    t0 = rbf(rbf(u0) * SC) * L2E;
    t1 = rbf(rbf(u1) * SC) * L2E;
  }
  {
    float m = fmaxf(t0, t1);
    m = fmaxf(m, dppf<ROR4>(m)); m = fmaxf(m, dppf<ROR8>(m));
    m = x16max(m);
    if (lane == 0) *(LAS float*)(L + L_MX + wv * 4) = m;
  }
  BARRIER();
  STAMP(6);
  {
    const u32 vo = (r * 128 + 16 * wv + (lane >> 3)) * 1024 + xcd * 128 + (lane & 7) * 16;
    DMA16(vo, wo, ldsb + L_WO + wv * 2048, W_NT);
    const u32 vo2 = vo + 8 * 1024;
    DMA16(vo2, wo, ldsb + L_WO + wv * 2048 + 1024, W_NT);
    const u32 lw2 = __builtin_amdgcn_readfirstlane(ldsb + L_W2 + wv * 512 * nk);
    const u32 vw = (kap2 * 32 + wv * 4 * nk + (lane >> 3)) * 1024 + xcd * 128 + (lane & 7) * 16;
    asm volatile("" :: "s"(lw2));
    if (lane < 32 * nk) {
      DMA16(vw, w2, lw2, W_NT);
    }
  }
  float M;
  v4u AV[8];
  {
    const v4f ma = *(const LAS v4f*)(L + L_MX), mb = *(const LAS v4f*)(L + L_MX + 16);
    M = fmaxf(fmaxf(fmaxf(ma[0], ma[1]), fmaxf(ma[2], ma[3])), fmaxf(fmaxf(mb[0], mb[1]), fmaxf(mb[2], mb[3])));
    const float p0 = __builtin_amdgcn_exp2f(t0 - M), p1 = __builtin_amdgcn_exp2f(t1 - M);
    float ls = p0 + p1;
    ls += dppf<ROR4>(ls); ls += dppf<ROR8>(ls);
    ls = x16sum(ls);
    if (lane == 0) *(LAS float*)(L + L_SM + wv * 4) = ls;
    const u32 pp0 = pk2(p0, p0), pp1 = pk2(p1, p1);
    const u32 hm = (lane & 1) ? 0xffff0000u : 0x0000ffffu, kd = (lane & 7) >> 1;
    const u32 mk0 = kd == 0 ? hm : 0u, mk1 = kd == 1 ? hm : 0u, mk2 = kd == 2 ? hm : 0u, mk3 = kd == 3 ? hm : 0u;
#define AVB(t, pp, c) { const u32 z = dppu<BC0 + c>(pp); AV[t] = v4u{z & mk0, z & mk1, z & mk2, z & mk3}; }
    AVB(0, pp0, 0) AVB(1, pp0, 4) AVB(2, pp0, 8) AVB(3, pp0, 12) AVB(4, pp1, 0) AVB(5, pp1, 4) AVB(6, pp1, 8) AVB(7, pp1, 12)
#undef AVB
    asm volatile("" : "+v"(AV[0]), "+v"(AV[1]), "+v"(AV[2]), "+v"(AV[3]), "+v"(AV[4]), "+v"(AV[5]), "+v"(AV[6]), "+v"(AV[7]));
  }
  {
    v4f d = {0.f, 0.f, 0.f, 0.f};
#define PVS(t, n) asm volatile("s_waitcnt vmcnt(" #n ") ; GUARD V" #t : "+v"(V[t]) :: "memory"); d = mfma16(AV[t], V[t], d); __builtin_amdgcn_sched_barrier(0);
    PVS(0, 10) PVS(1, 9) PVS(2, 8) PVS(3, 7) PVS(4, 6) PVS(5, 5) PVS(6, 4) PVS(7, 3)
#undef PVS
    if (lane < 32) *(LAS v4f*)(L + L_RED + wv * 512 + (8 * i16 + 4 * g) * 4) = d;
  }
  BARRIER();
  STAMP(7);
  v4u P0, P1, P2, P3, P4, P5, P6, P7, PM;
  const u32 voP = WS_OP + r * 8 * 1152 + lane * 16 + 4032, voM = WS_OP + (r * 8 + (lane & 7)) * 1152 + 1024;
  if (wv == 0) {
    float o0 = 0.f, o1 = 0.f;
#pragma unroll
    for (int k = 0; k < 8; k++) { const v2f v = *(const LAS v2f*)(L + L_RED + k * 512 + lane * 8); o0 += v.x; o1 += v.y; }
    const u32 vo = WS_OP + (h * 8 + j) * 1152 + lane * 16;
    const v4u gq = {__float_as_uint(o0), tag, __float_as_uint(o1), tag};
    asm volatile("global_store_dwordx4 %0, %1, %2 sc1\n\ts_nop 1" :: "v"(vo), "v"(gq), "s"(ws) : "memory");
    VMWAIT(1);
  } else if (wv == 1) {
    const v4f sa = *(const LAS v4f*)(L + L_SM), sb = *(const LAS v4f*)(L + L_SM + 16);
    const float ls = (((sa[0] + sa[1]) + (sa[2] + sa[3])) + ((sb[0] + sb[1]) + (sb[2] + sb[3])));
    if (lane == 0) {
      const u32 vo = WS_OP + (h * 8 + j) * 1152 + 1024;
      const v4u gq = {__float_as_uint(M), tag, __float_as_uint(ls), tag};
      asm volatile("global_store_dwordx4 %0, %1, %2 sc1\n\ts_nop 1" :: "v"(vo), "v"(gq), "s"(ws) : "memory");
    }
    VMWAIT(1);
  } else if (wv == 2) {
    __builtin_amdgcn_s_sleep(DLY1);
    asm volatile(
        "global_load_dwordx4 %8, %10, %11 sc1 ; LD PM\n\t"
        "global_load_dwordx4 %0, %9, %11 offset:-4032 sc1 ; LD P0\n\t"
        "global_load_dwordx4 %1, %9, %11 offset:-2880 sc1 ; LD P1\n\t"
        "global_load_dwordx4 %2, %9, %11 offset:-1728 sc1 ; LD P2\n\t"
        "global_load_dwordx4 %3, %9, %11 offset:-576 sc1 ; LD P3\n\t"
        "global_load_dwordx4 %4, %9, %11 offset:576 sc1 ; LD P4\n\t"
        "global_load_dwordx4 %5, %9, %11 offset:1728 sc1 ; LD P5\n\t"
        "global_load_dwordx4 %6, %9, %11 offset:2880 sc1 ; LD P6\n\t"
        "global_load_dwordx4 %7, %9, %11 offset:4032 sc1 ; LD P7\n\t"
        "s_waitcnt vmcnt(9)"
        : "=&v"(P0), "=&v"(P1), "=&v"(P2), "=&v"(P3), "=&v"(P4), "=&v"(P5), "=&v"(P6), "=&v"(P7), "=&v"(PM)
        : "v"(voP), "v"(voM), "s"(ws) : "memory");
  } else {
    VMWAIT(0);
  }
  BARRIER();
  STAMP(8);
  v4u BW[4];
  v4f dg = {0.f, 0.f, 0.f, 0.f};
  if (wv >= 4) {
    const u32 t = wv - 4;
    const u32 boff = (8 * g + trq) * 128 + t * 32 + trp * 8;
#pragma unroll
    for (int ks = 0; ks < 4; ks++) BW[ks] = trfrag(L, L_WO + boff + ks * 4096, 512);
    for (u32 ki = 0; ki < nk; ki++)
      dg = mfma16(lds128(L, L_HB + (lane & 1) * 128 + ki * 64 + g * 16), trfrag(L, L_W2 + boff + ki * 4096, 512), dg);
  } else if (wv == 2) {
    asm volatile("s_waitcnt vmcnt(0) ; GUARD P0 P1 P2 P3 P4 P5 P6 P7 PM" : "+v"(P0), "+v"(P1), "+v"(P2), "+v"(P3), "+v"(P4), "+v"(P5), "+v"(P6), "+v"(P7), "+v"(PM) :: "memory");
    v4u p0 = P0, p1 = P1, p2 = P2, p3 = P3, p4 = P4, p5 = P5, p6 = P6, p7 = P7, pm = PM;
    for (u32 spin = 0;; spin++) {
      const u32 bad = ((p0.y ^ tag) | (p0.w ^ tag) | (p1.y ^ tag) | (p1.w ^ tag)) | ((p2.y ^ tag) | (p2.w ^ tag) | (p3.y ^ tag) | (p3.w ^ tag)) |
                      ((p4.y ^ tag) | (p4.w ^ tag) | (p5.y ^ tag) | (p5.w ^ tag)) | ((p6.y ^ tag) | (p6.w ^ tag) | (p7.y ^ tag) | (p7.w ^ tag)) |
                      ((pm.y ^ tag) | (pm.w ^ tag));
      if (__all(bad == 0) || spin > SPIN_CAP) break;
      asm volatile(
          "global_load_dwordx4 %8, %10, %11 sc1\n\t"
          "global_load_dwordx4 %0, %9, %11 offset:-4032 sc1\n\t"
          "global_load_dwordx4 %1, %9, %11 offset:-2880 sc1\n\t"
          "global_load_dwordx4 %2, %9, %11 offset:-1728 sc1\n\t"
          "global_load_dwordx4 %3, %9, %11 offset:-576 sc1\n\t"
          "global_load_dwordx4 %4, %9, %11 offset:576 sc1\n\t"
          "global_load_dwordx4 %5, %9, %11 offset:1728 sc1\n\t"
          "global_load_dwordx4 %6, %9, %11 offset:2880 sc1\n\t"
          "global_load_dwordx4 %7, %9, %11 offset:4032 sc1\n\t"
          "s_waitcnt vmcnt(0)"
          : "=&v"(p0), "=&v"(p1), "=&v"(p2), "=&v"(p3), "=&v"(p4), "=&v"(p5), "=&v"(p6), "=&v"(p7), "=&v"(pm)
          : "v"(voP), "v"(voM), "s"(ws) : "memory");
    }
    STAMP(9);
    const float mp = __uint_as_float(pm.x), lp = __uint_as_float(pm.z);
    float Mb = fmaxf(mp, dppf<XOR1>(mp)); Mb = fmaxf(Mb, dppf<XOR2>(Mb));
    const float e = __builtin_amdgcn_exp2f(mp - Mb);
    float Lb = e * lp; Lb += dppf<XOR1>(Lb); Lb += dppf<XOR2>(Lb);
    const float f = e * __builtin_amdgcn_rcpf(Lb);
    const float f0 = dppf<BC0 + 0>(f), f1 = dppf<BC0 + 1>(f), f2 = dppf<BC0 + 2>(f), f3 = dppf<BC0 + 3>(f);
    const float f4 = dppf<BC0 + 4>(f), f5 = dppf<BC0 + 5>(f), f6 = dppf<BC0 + 6>(f), f7 = dppf<BC0 + 7>(f);
    const float a0 = fmaf(f0, __uint_as_float(p0.x), f1 * __uint_as_float(p1.x)) + fmaf(f2, __uint_as_float(p2.x), f3 * __uint_as_float(p3.x));
    const float a1 = fmaf(f0, __uint_as_float(p0.z), f1 * __uint_as_float(p1.z)) + fmaf(f2, __uint_as_float(p2.z), f3 * __uint_as_float(p3.z));
    const float c0 = fmaf(f4, __uint_as_float(p4.x), f5 * __uint_as_float(p5.x)) + fmaf(f6, __uint_as_float(p6.x), f7 * __uint_as_float(p7.x));
    const float c1 = fmaf(f4, __uint_as_float(p4.z), f5 * __uint_as_float(p5.z)) + fmaf(f6, __uint_as_float(p6.z), f7 * __uint_as_float(p7.z));
    *(LAS u32*)(L + L_ON + lane * 4) = pk2(a0, a1);
    *(LAS u32*)(L + L_ON + 256 + lane * 4) = pk2(c0, c1);
  }
  BARRIER();
  STAMP(10);
  if (wv >= 4) {
    const u32 t = wv - 4;
    const u32 aoff = L_ON + (lane & 1) * 256 + g * 16;
    v4f d = {0.f, 0.f, 0.f, 0.f};
#pragma unroll
    for (int ks = 0; ks < 4; ks++) d = mfma16(lds128(L, aoff + ks * 64), BW[ks], d);
    auto ss = __builtin_amdgcn_permlane16_swap(__float_as_uint(d[0]), __float_as_uint(d[1]), false, false);
    auto gg = __builtin_amdgcn_permlane16_swap(__float_as_uint(dg[0]), __float_as_uint(dg[1]), false, false);
    if (lane < 32) {
      const u32 vo = WS_SP + ((xcd * 32 + r) * 128 + g * 64 + 16 * t + i16) * 16;
      const v4u gq = {ss[0], tag, gg[0], tag};
      asm volatile("global_store_dwordx4 %0, %1, %2 sc1\n\ts_nop 1" :: "v"(vo), "v"(gq), "s"(ws) : "memory");
    }
    STAMP(11);
  } else if (wv == 0 && r < 16) {
    const u32 bb = r >> 3, col = xcd * 64 + (r & 7) * 8 + (lane & 7);
    const u32 vo = WS_SP + ((xcd * 32 + (lane >> 3)) * 128 + bb * 64 + (r & 7) * 8 + (lane & 7)) * 16;
    const u32 vo1 = vo + 8 * 2048, vo2 = vo + 16 * 2048, vo3 = vo + 24 * 2048;
    const u32 vx = (bb * 512 + col) * 2, vb = col * 2;
    u32 xv, bv;
    asm volatile("global_load_ushort %0, %2, %4 ; LD XV\n\tglobal_load_ushort %1, %3, %5 ; LD BV" : "=&v"(xv), "=&v"(bv) : "v"(vx), "v"(vb), "s"(x), "s"(b2) : "memory");
    __builtin_amdgcn_s_sleep(DLY2);
    float s, gs;
    for (u32 spin = 0;; spin++) {
      v4u p0, p1, p2, p3;
      asm volatile(
          "global_load_dwordx4 %0, %4, %8 sc1\n\t"
          "global_load_dwordx4 %1, %5, %8 sc1\n\t"
          "global_load_dwordx4 %2, %6, %8 sc1\n\t"
          "global_load_dwordx4 %3, %7, %8 sc1\n\t"
          "s_waitcnt vmcnt(0)"
          : "=&v"(p0), "=&v"(p1), "=&v"(p2), "=&v"(p3)
          : "v"(vo), "v"(vo1), "v"(vo2), "v"(vo3), "s"(ws) : "memory");
      const u32 bad = ((p0.y ^ tag) | (p0.w ^ tag) | (p1.y ^ tag) | (p1.w ^ tag)) | ((p2.y ^ tag) | (p2.w ^ tag) | (p3.y ^ tag) | (p3.w ^ tag));
      s = (__uint_as_float(p0.x) + __uint_as_float(p1.x)) + (__uint_as_float(p2.x) + __uint_as_float(p3.x));
      gs = (__uint_as_float(p0.z) + __uint_as_float(p1.z)) + (__uint_as_float(p2.z) + __uint_as_float(p3.z));
      if (__all(bad == 0) || spin > SPIN_CAP) break;
    }
    asm volatile("; GUARD XV BV" : "+v"(xv), "+v"(bv));
    s += dppf<ROR8>(s); gs += dppf<ROR8>(gs);
    s = x16sum(s); gs = x16sum(gs);
    const float gt = rbf(rbf(gs) + bflo(bv));
    const float sg = rbf(gt * __builtin_amdgcn_rcpf(1.f + __builtin_amdgcn_exp2f(gt * -1.4426950408889634f)));
    const float o = bflo(xv) + rbf(rbf(s) * sg);
    if (lane < 8) {
      const u32 vo_ = (bb * 512 + col) * 2;
      u32 ob = pk2(o, o);
      asm volatile("global_store_short %0, %1, %2" :: "v"(vo_), "v"(ob), "s"(out) : "memory");
    }
    if (w == 0 && lane == 0) {
      const u32 z = 0;
      asm volatile("global_store_dword %0, %1, %2 sc1" :: "v"(z), "v"(tag), "s"(ws) : "memory");
    }
    STAMP(12);
  }
}

extern "C" void kernel_launch(void* const* d_in, const int* in_sizes, int n_in, void* d_out, int out_size, void* d_ws, size_t ws_size,
                              hipStream_t stream) {
  fused_decode<<<256, NTHR, 0, stream>>>((const u16*)d_in[0], (const u16*)d_in[1], (const u16*)d_in[2], (const u16*)d_in[3], (const u16*)d_in[4],
                                        (const u16*)d_in[6], (const u16*)d_in[7], (const u16*)d_in[8], (const u16*)d_in[9], (u16*)d_out,
                                        (unsigned char*)d_ws);
}
```

```cpp
#include <hip/hip_runtime.h>
#include <stdint.h>

typedef unsigned u32;
typedef unsigned long long u64;
typedef unsigned short u16;
typedef u32 v4u __attribute__((ext_vector_type(4)));
typedef u32 v2u __attribute__((ext_vector_type(2)));
typedef float v4f __attribute__((ext_vector_type(4)));
typedef float v2f __attribute__((ext_vector_type(2)));
typedef short s8v __attribute__((ext_vector_type(8)));
typedef short s4v __attribute__((ext_vector_type(4)));
typedef __bf16 bf2v __attribute__((ext_vector_type(2)));
#define LAS __attribute__((address_space(3)))
#define CAS __attribute__((address_space(4)))

#define KV_NT " nt"
#define W_NT " nt"
constexpr int TAU = 36;
constexpr int NB0 = 2;
constexpr int WIN = 8;
constexpr int DTAU = 24;
constexpr int DLYK = 8, DLYQ = 36, DLYH1 = 22, DLYH2 = 16;
constexpr int CODE_KB = 11;
constexpr u32 SPIN_CAP = 200000;

constexpr u32 WS_QP = 4096;
constexpr u32 WS_HP = WS_QP + 32 * 8 * 2 * 1024;
constexpr u32 OPS = 640;
constexpr u32 WS_OP = WS_HP + 43 * 4 * 512 + 2048;
constexpr u32 WS_SP = WS_OP + 256 * OPS + 4096;
static_assert(WS_HP % 128 == 0 && WS_OP % 128 == 0 && WS_SP % 128 == 0, "line aligned");
constexpr u32 L_WQ = 0;
constexpr u32 L_W1 = 16384;
constexpr u32 L_WO = 24576;
constexpr u32 L_W2 = 40960;
constexpr u32 L_XN = 49152;
constexpr u32 L_QB = 51200;
constexpr u32 L_HB = 51456;
constexpr u32 L_ON = 51712;
constexpr u32 L_MX = 52224;
constexpr u32 L_SM = 52256;
constexpr u32 L_CT = 52288;
constexpr u32 L_FL = 52352;
constexpr u32 L_RED = 52416;
constexpr u32 L_BYTES = 86016;
enum { C_WQ = 0, C_W1 = 1, C_Q = 2, C_ML = 4, C_WO = 7, C_QP = 9, C_ISS = 11 };

__device__ __forceinline__ v4f mfma16(v4u a, v4u b, v4f c) {
  return __builtin_amdgcn_mfma_f32_16x16x32_bf16(__builtin_bit_cast(s8v, a), __builtin_bit_cast(s8v, b), c, 0, 0, 0);
}
__device__ __forceinline__ u32 pk2(float lo, float hi) { bf2v z = {(__bf16)lo, (__bf16)hi}; return __builtin_bit_cast(u32, z); }
__device__ __forceinline__ float rbf(float x) { return __uint_as_float(pk2(x, x) & 0xffff0000u); }
__device__ __forceinline__ float bflo(u32 v) { return __uint_as_float(v << 16); }
__device__ __forceinline__ float bfhi(u32 v) { return __uint_as_float(v & 0xffff0000u); }
template <int CTRL> __device__ __forceinline__ float dppf(float x) {
  return __builtin_bit_cast(float, __builtin_amdgcn_mov_dpp(__builtin_bit_cast(int, x), CTRL, 0xf, 0xf, true));
}
template <int CTRL> __device__ __forceinline__ u32 dppu(u32 x) { return (u32)__builtin_amdgcn_mov_dpp((int)x, CTRL, 0xf, 0xf, true); }
constexpr int XOR1 = 0xB1, XOR2 = 0x4E, HMIR = 0x141, MIR = 0x140, ROR4 = 0x124, ROR8 = 0x128, BC0 = 0x150;
__device__ __forceinline__ float fmx(float a, float b) { return __builtin_amdgcn_fmed3f(a, b, __builtin_inff()); }
__device__ __forceinline__ float x16sum(float x) {
  auto s = __builtin_amdgcn_permlane16_swap(__float_as_uint(x), __float_as_uint(x), false, false);
  x = __uint_as_float(s[0]) + __uint_as_float(s[1]);
  auto t = __builtin_amdgcn_permlane32_swap(__float_as_uint(x), __float_as_uint(x), false, false);
  return __uint_as_float(t[0]) + __uint_as_float(t[1]);
}
__device__ __forceinline__ float x16max(float x) {
  auto s = __builtin_amdgcn_permlane16_swap(__float_as_uint(x), __float_as_uint(x), false, false);
  x = fmx(__uint_as_float(s[0]), __uint_as_float(s[1]));
  auto t = __builtin_amdgcn_permlane32_swap(__float_as_uint(x), __float_as_uint(x), false, false);
  return fmx(__uint_as_float(t[0]), __uint_as_float(t[1]));
}
__device__ __forceinline__ float wave_sum(float x) {
  x += dppf<XOR1>(x); x += dppf<XOR2>(x); x += dppf<HMIR>(x); x += dppf<MIR>(x);
  return x16sum(x);
}
__device__ __forceinline__ u32 umin3(u32 a, u32 b, u32 c) { return min(min(a, b), c); }
__device__ __forceinline__ u32 umax3(u32 a, u32 b, u32 c) { return max(max(a, b), c); }
__device__ __forceinline__ u32 now32() { return (u32)__builtin_amdgcn_s_memrealtime(); }
#define VMWAIT(n) asm volatile("s_waitcnt vmcnt(" #n ")" ::: "memory")

#define DMA16(voff, base, lds, POL) \
  asm volatile("s_mov_b32 m0, %2\n\ts_nop 0\n\tglobal_load_lds_dwordx4 %0, %1" POL :: "v"(voff), "s"(base), "s"(lds) : "memory")
#define LD16(dst, voff, base, imm, POL, NAME) \
  asm volatile("global_load_dwordx4 %0, %1, %2 offset:" #imm POL " ; LD " NAME : "=v"(dst) : "v"(voff), "s"(base) : "memory")

__device__ __forceinline__ v4u lds128(const LAS unsigned char* L, u32 off) { return *(const LAS v4u*)(L + off); }
__device__ __forceinline__ v4u trfrag(const LAS unsigned char* L, u32 off, u32 step4) {
  s4v lo = __builtin_amdgcn_ds_read_tr16_b64_v4i16((LAS s4v*)(L + off));
  s4v hi = __builtin_amdgcn_ds_read_tr16_b64_v4i16((LAS s4v*)(L + off + step4));
  v2u a = __builtin_bit_cast(v2u, lo), b = __builtin_bit_cast(v2u, hi);
  return v4u{a.x, a.y, b.x, b.y};
}
__device__ __forceinline__ float maxq(float x) {
  float a, c;
  asm("s_nop 1\n\tv_max_f32_dpp %0, %1, %1 quad_perm:[1,0,3,2] row_mask:0xf bank_mask:0xf" : "=v"(a) : "v"(x));
  asm("s_nop 1\n\tv_max_f32_dpp %0, %1, %1 quad_perm:[2,3,0,1] row_mask:0xf bank_mask:0xf" : "=v"(c) : "v"(a));
  return c;
}
__device__ __forceinline__ void factors(v4u pm, float (&F)[8]) {
  const float mp = __uint_as_float(pm.x), lp = __uint_as_float(pm.z);
  const float e = __builtin_amdgcn_exp2f(mp - maxq(mp));
  float Lb = e * lp; Lb += dppf<XOR1>(Lb); Lb += dppf<XOR2>(Lb);
  const float f = e * __builtin_amdgcn_rcpf(Lb);
  F[0] = dppf<BC0 + 0>(f); F[1] = dppf<BC0 + 1>(f); F[2] = dppf<BC0 + 2>(f); F[3] = dppf<BC0 + 3>(f);
  F[4] = dppf<BC0 + 4>(f); F[5] = dppf<BC0 + 5>(f); F[6] = dppf<BC0 + 6>(f); F[7] = dppf<BC0 + 7>(f);
}
__device__ __forceinline__ void ct_add(LAS unsigned char* L, u32 idx, u32 lane) {
  asm volatile("s_waitcnt lgkmcnt(0)" ::: "memory");
  if (lane == 0) __hip_atomic_fetch_add((LAS u32*)(L + L_CT + idx * 4), 1u, __ATOMIC_RELAXED, __HIP_MEMORY_SCOPE_WORKGROUP);
  asm volatile("" ::: "memory");
}
__device__ __forceinline__ void ct_wait(LAS unsigned char* L, u32 idx, u32 n) {
  while (__builtin_amdgcn_readfirstlane(*(volatile LAS u32*)(L + L_CT + idx * 4)) < n) __builtin_amdgcn_s_sleep(1);
  asm volatile("" ::: "memory");
}
__device__ __forceinline__ void red_spin(LAS unsigned char* L) {
  for (;;) {
    const v4u a = *(volatile LAS v4u*)(L + L_FL), c = *(volatile LAS v4u*)(L + L_FL + 16);
    if (__builtin_amdgcn_readfirstlane((a.x & a.y) & (a.z & a.w) & (c.x & c.y) & (c.z & c.w)) != 0u) break;
  }
  asm volatile("" ::: "memory");
}
__device__ __forceinline__ void ct_spin(LAS unsigned char* L, u32 idx, u32 n) {
  while (__builtin_amdgcn_readfirstlane(*(volatile LAS u32*)(L + L_CT + idx * 4)) < n) {}
  asm volatile("" ::: "memory");
}

__global__ void __launch_bounds__(768, 3)
fused_decode(const u16* __restrict__ x, const u16* __restrict__ kc, const u16* __restrict__ vc, const u16* __restrict__ wq,
             const u16* __restrict__ wo, const u16* __restrict__ w1, const u16* __restrict__ b1, const u16* __restrict__ w2,
             const u16* __restrict__ b2, u16* __restrict__ out, unsigned char* __restrict__ ws) {
  __shared__ __attribute__((aligned(16))) unsigned char smem[L_BYTES];
  LAS unsigned char* L = (LAS unsigned char*)smem;
  const u32 ldsb = (u32)(size_t)smem;
  const u32 wv = __builtin_amdgcn_readfirstlane(threadIdx.x >> 6);
  u32 pf0, pf1, pf2;
  if (wv == 3) {
    u64 pc; asm volatile("s_getpc_b64 %0" : "=s"(pc));
    pc &= ~(u64)255;
    const u32 vo = (threadIdx.x & 63) * 64;
    const u32 lim = CODE_KB * 1024 - 64;
    const u32 v0 = vo, v1 = min(vo + 4096u, lim), v2 = min(vo + 8192u, lim);
    asm volatile("global_load_dword %0, %3, %6\n\tglobal_load_dword %1, %4, %6\n\tglobal_load_dword %2, %5, %6"
                 : "=&v"(pf0), "=&v"(pf1), "=&v"(pf2) : "v"(v0), "v"(v1), "v"(v2), "s"(pc) : "memory");
  }
  asm volatile("" :: "s"(x), "s"(kc), "s"(vc), "s"(wq), "s"(wo), "s"(w1), "s"(b1), "s"(w2), "s"(b2), "s"(out), "s"(ws));
  const u32 lane = threadIdx.x & 63;
  const u32 w = blockIdx.x, xcd = w & 7, r = w >> 3;
  const u32 h = 4 * xcd + (r >> 3), j = r & 7, b = j >> 2, sp = j & 3;
  const u32 g = lane >> 4, i16 = lane & 15;

  if (wv >= 4) {
    const u32 sw = wv - 4;
    const u32 kvbase = ((b * 32 + h) * 4096 + 3072 + sp * 256 + sw * 32) * 256 + 4096;
    const u32 vK = kvbase + ((lane >> 2) & 3) * 256 + (4 * g + (lane & 3)) * 16;
    const u32 vV = kvbase + g * 1024 + i16 * 16;
    v4u K[8], V[8];
    asm volatile("" :: "v"(vK), "v"(vV));
    asm volatile("s_barrier" ::: "memory");
    const u32 tb = now32() + (DLYK * 64) / 24;
#define PT(n) do { if ((n) >= NB0) { while ((int)(now32() - (tb + ((n) - NB0 + 1) * TAU)) < 0) {} } \
                   if ((n) >= WIN) asm volatile("s_waitcnt vmcnt(%0)" :: "i"(WIN - 1) : "memory"); } while (0)
    while ((int)(now32() - tb) < 0) {}
    LD16(K[0], vK, kc, -4096, KV_NT, "K0");
    PT(1); LD16(K[1], vK, kc, -3072, KV_NT, "K1");
    u32 epoch4;
    asm volatile("s_load_dword %0, %1, 0x0\n\ts_waitcnt lgkmcnt(0)" : "=s"(epoch4) : "s"(ws) : "memory");
    PT(2); LD16(K[2], vK, kc, -2048, KV_NT, "K2");
    PT(3); LD16(K[3], vK, kc, -1024, KV_NT, "K3");
    PT(4); LD16(K[4], vK, kc, 0, KV_NT, "K4");
    PT(5); LD16(K[5], vK, kc, 1024, KV_NT, "K5");
    PT(6); LD16(K[6], vK, kc, 2048, KV_NT, "K6");
    PT(7); LD16(K[7], vK, kc, 3072, KV_NT, "K7");
    PT(8); LD16(V[0], vV, vc, -4096, KV_NT, "V0");
    PT(9); LD16(V[1], vV, vc, -3840, KV_NT, "V1");
    PT(10); LD16(V[2], vV, vc, -3584, KV_NT, "V2");
    PT(11); LD16(V[3], vV, vc, -3328, KV_NT, "V3");
    PT(12); LD16(V[4], vV, vc, 0, KV_NT, "V4");
    ct_wait(L, C_Q, 1);
    float t0, t1;
    {
      const v4u qc = lds128(L, L_QB + (4 * g + (lane & 3)) * 16);
      v4u A[4];
#pragma unroll
      for (int a = 0; a < 4; a++) {
        const bool on = (i16 >> 2) == (u32)a;
        A[a] = v4u{on ? qc.x : 0u, on ? qc.y : 0u, on ? qc.z : 0u, on ? qc.w : 0u};
      }
      asm volatile("s_waitcnt vmcnt(5) ; GUARD K0 K1 K2 K3 K4 K5 K6 K7"
                   : "+v"(K[0]), "+v"(K[1]), "+v"(K[2]), "+v"(K[3]), "+v"(K[4]), "+v"(K[5]), "+v"(K[6]), "+v"(K[7]) :: "memory");
      v4f d0 = {0.f, 0.f, 0.f, 0.f}, d1 = {0.f, 0.f, 0.f, 0.f};
#pragma unroll
      for (int a = 0; a < 4; a++) { d0 = mfma16(A[a], K[a], d0); d1 = mfma16(A[a], K[4 + a], d1); }
      const bool s1 = lane & 1, s2 = lane & 2;
      float u0 = s2 ? (s1 ? d0[3] : d0[2]) : (s1 ? d0[1] : d0[0]);
      float u1 = s2 ? (s1 ? d1[3] : d1[2]) : (s1 ? d1[1] : d1[0]);
      u0 += dppf<XOR1>(u0); u1 += dppf<XOR1>(u1);
      u0 += dppf<XOR2>(u0); u1 += dppf<XOR2>(u1);
      constexpr float SC = 0.08838834764831845f, L2E = 1.4426950408889634f;
      t0 = rbf(rbf(u0) * SC) * L2E;
      t1 = rbf(rbf(u1) * SC) * L2E;
      asm volatile("" : "+v"(t0), "+v"(t1));
    }
    PT(13); LD16(V[5], vV, vc, 256, KV_NT, "V5");
    u32 pp0, pp1;
    {
      float m = fmx(t0, t1);
      m = fmx(m, dppf<ROR4>(m)); m = fmx(m, dppf<ROR8>(m));
      m = x16max(m);
      const float p0 = __builtin_amdgcn_exp2f(t0 - m), p1 = __builtin_amdgcn_exp2f(t1 - m);
      float ls = p0 + p1;
      ls += dppf<ROR4>(ls); ls += dppf<ROR8>(ls);
      ls = x16sum(ls);
      if (lane == 0) { *(LAS float*)(L + L_MX + sw * 4) = m; *(LAS float*)(L + L_SM + sw * 4) = ls; }
      ct_add(L, C_ML, lane);
      pp0 = pk2(p0, p0); pp1 = pk2(p1, p1);
      asm volatile("" : "+v"(pp0), "+v"(pp1));
    }
    PT(14); LD16(V[6], vV, vc, 512, KV_NT, "V6");
    v4u AV[8];
    {
      const u32 hm = (lane & 1) ? 0xffff0000u : 0x0000ffffu, kd = (lane & 7) >> 1;
      const u32 mk0 = kd == 0 ? hm : 0u, mk1 = kd == 1 ? hm : 0u, mk2 = kd == 2 ? hm : 0u, mk3 = kd == 3 ? hm : 0u;
#define AVB(t, pp, c) { const u32 z = dppu<BC0 + c>(pp); AV[t] = v4u{z & mk0, z & mk1, z & mk2, z & mk3}; }
      AVB(0, pp0, 0) AVB(1, pp0, 4) AVB(2, pp0, 8) AVB(3, pp0, 12) AVB(4, pp1, 0) AVB(5, pp1, 4) AVB(6, pp1, 8) AVB(7, pp1, 12)
#undef AVB
      asm volatile("" : "+v"(AV[0]), "+v"(AV[1]), "+v"(AV[2]), "+v"(AV[3]), "+v"(AV[4]), "+v"(AV[5]), "+v"(AV[6]), "+v"(AV[7]));
    }
    PT(15); LD16(V[7], vV, vc, 768, KV_NT, "V7");
#undef PT
    if (lane == 0) __hip_atomic_fetch_add((LAS u32*)(L + L_CT + C_ISS * 4), 1u, __ATOMIC_RELAXED, __HIP_MEMORY_SCOPE_WORKGROUP);
    v4f d = {0.f, 0.f, 0.f, 0.f};
#define PVS(t, n) asm volatile("s_waitcnt vmcnt(" #n ") ; GUARD V" #t : "+v"(V[t]) :: "memory"); d = mfma16(AV[t], V[t], d); __builtin_amdgcn_sched_barrier(0);
    PVS(0, 7) PVS(1, 6) PVS(2, 5) PVS(3, 4) PVS(4, 3) PVS(5, 2) PVS(6, 1) PVS(7, 0)
#undef PVS
    if (lane < 32) *(LAS v4f*)(L + L_RED + sw * 512 + (8 * i16 + 4 * g) * 4) = d;
    asm volatile("s_waitcnt lgkmcnt(0)\n\tds_write_b32 %0, %1" :: "v"(ldsb + L_FL + sw * 4), "v"(1u) : "memory");
    if (wv == 4 && r < 16) {
      const u32 tag = epoch4 + 1u;
      const u32 bb = r >> 3, col = xcd * 64 + (r & 7) * 8 + (lane & 7);
      const u32 vo = WS_SP + ((xcd * 32 + (lane >> 3)) * 128 + bb * 64 + (r & 7) * 8 + (lane & 7)) * 16;
      const u32 vo1 = vo + 8 * 2048, vo2 = vo + 16 * 2048, vo3 = vo + 24 * 2048;
      const u32 vx = (bb * 512 + col) * 2, vb = col * 2;
      u32 xv, bv;
      asm volatile("global_load_ushort %0, %2, %4\n\tglobal_load_ushort %1, %3, %5\n\ts_waitcnt vmcnt(0)" : "=&v"(xv), "=&v"(bv) : "v"(vx), "v"(vb), "s"(x), "s"(b2) : "memory");
      asm volatile("" :: "v"(vo), "v"(vo1), "v"(vo2), "v"(vo3));
      asm volatile("s_barrier" ::: "memory");
      __builtin_amdgcn_s_sleep(DLYH2);
      float s, gs;
      for (u32 spin = 0;; spin++) {
        v4u p0, p1, p2, p3;
        asm volatile(
            "global_load_dwordx4 %0, %4, %8 sc1\n\t"
            "global_load_dwordx4 %1, %5, %8 sc1\n\t"
            "global_load_dwordx4 %2, %6, %8 sc1\n\t"
            "global_load_dwordx4 %3, %7, %8 sc1\n\t"
            "s_waitcnt vmcnt(0)"
            : "=&v"(p0), "=&v"(p1), "=&v"(p2), "=&v"(p3)
            : "v"(vo), "v"(vo1), "v"(vo2), "v"(vo3), "s"(ws) : "memory");
        const u32 mn = umin3(umin3(p0.y, p0.w, p1.y), umin3(p1.w, p2.y, p2.w), umin3(p3.y, p3.w, tag));
        const u32 mx = umax3(umax3(p0.y, p0.w, p1.y), umax3(p1.w, p2.y, p2.w), umax3(p3.y, p3.w, tag));
        s = (__uint_as_float(p0.x) + __uint_as_float(p1.x)) + (__uint_as_float(p2.x) + __uint_as_float(p3.x));
        gs = (__uint_as_float(p0.z) + __uint_as_float(p1.z)) + (__uint_as_float(p2.z) + __uint_as_float(p3.z));
        if (__all(mn == mx) || spin > SPIN_CAP) break;
      }
      s += dppf<ROR8>(s); gs += dppf<ROR8>(gs);
      s = x16sum(s); gs = x16sum(gs);
      const float gt = rbf(rbf(gs) + bflo(bv));
      const float sg = rbf(gt * __builtin_amdgcn_rcpf(1.f + __builtin_amdgcn_exp2f(gt * -1.4426950408889634f)));
      const float o = bflo(xv) + rbf(rbf(s) * sg);
      if (lane < 8) {
        const u32 vo_ = (bb * 512 + col) * 2;
        const u32 ob = pk2(o, o);
        asm volatile("global_store_short %0, %1, %2" :: "v"(vo_), "v"(ob), "s"(out) : "memory");
      }
      if (w == 0 && lane == 0) {
        const u32 z = 0;
        asm volatile("global_store_dword %0, %1, %2 sc1" :: "v"(z), "v"(tag), "s"(ws) : "memory");
      }
    }
    return;
  }

  const u32 cmb = (r >> 1) * 8 + xcd, kap = 2 * (cmb >> 2) + (r & 1), kq = cmb & 3;
  const bool ffn = (r < 22) && (kap < 43);
  const u32 nk = r < 21 ? 1 : 2, kap2 = r < 21 ? r : 21 + 2 * (r - 21);
  v4u xr;
  if (wv < 2) { const u32 vo = wv * 1024 + lane * 16; LD16(xr, vo, x, 0, "", "X"); }
  {
    const u32 vo = (64 * j + 16 * wv + g) * 8192 + h * 256 + i16 * 16;
    const u32 ld = ldsb + L_WQ + wv * 4096;
    DMA16(vo, wq, ld, W_NT);
    const u32 vo1 = vo + 4 * 8192, vo2 = vo + 8 * 8192, vo3 = vo + 12 * 8192;
    DMA16(vo1, wq, ld + 1024, W_NT);
    DMA16(vo2, wq, ld + 2048, W_NT);
    DMA16(vo3, wq, ld + 3072, W_NT);
  }
  if (wv == 3 && lane < 24) *(LAS u32*)(L + L_CT + lane * 4) = 0u;
  asm volatile("s_waitcnt lgkmcnt(0)\n\ts_barrier" ::: "memory");
  u32 epoch;
  asm volatile("s_load_dword %0, %1, 0x0" : "=s"(epoch) : "s"(ws) : "memory");
  if (wv < 2) {
    asm volatile("s_waitcnt vmcnt(4) ; GUARD X" : "+v"(xr) :: "memory");
    float v[8] = {bflo(xr.x), bfhi(xr.x), bflo(xr.y), bfhi(xr.y), bflo(xr.z), bfhi(xr.z), bflo(xr.w), bfhi(xr.w)};
    float s = ((v[0] + v[1]) + (v[2] + v[3])) + ((v[4] + v[5]) + (v[6] + v[7]));
    const float mean = wave_sum(s) * (1.f / 512.f);
    float q = 0.f;
#pragma unroll
    for (int k = 0; k < 8; k++) { const float d = v[k] - mean; q = fmaf(d, d, q); }
    const float var = wave_sum(q) * (1.f / 512.f);
    const float m = rbf(mean);
    const float sd = rbf(__builtin_amdgcn_sqrtf(rbf(rbf(var) + 1e-5f)));
    const float inv = __builtin_amdgcn_rcpf(sd);
    v4u o;
    o.x = pk2(rbf(v[0] - m) * inv, rbf(v[1] - m) * inv);
    o.y = pk2(rbf(v[2] - m) * inv, rbf(v[3] - m) * inv);
    o.z = pk2(rbf(v[4] - m) * inv, rbf(v[5] - m) * inv);
    o.w = pk2(rbf(v[6] - m) * inv, rbf(v[7] - m) * inv);
    *(LAS v4u*)(L + L_XN + wv * 1024 + lane * 16) = o;
  }
  VMWAIT(0);
  asm volatile("" : "+v"(pf0), "+v"(pf1), "+v"(pf2));
  asm volatile("s_waitcnt lgkmcnt(0)" : "+s"(epoch) :: "memory");
  const u32 tag = epoch + 1u;
  ct_add(L, C_WQ, lane);
  ct_wait(L, C_WQ, 4);
  const u32 trq = (i16 >> 2), trp = lane & 3;
  {
    const u32 aoff = L_XN + (lane & 1) * 1024 + (64 * j + 8 * g) * 2;
    const u32 boff = L_WQ + (8 * g + trq) * 256 + wv * 64 + trp * 8;
    const v4u a0 = lds128(L, aoff), a1 = lds128(L, aoff + 64);
    v4f d = {0.f, 0.f, 0.f, 0.f}, e = {0.f, 0.f, 0.f, 0.f};
    d = mfma16(a0, trfrag(L, boff, 1024), d);
    e = mfma16(a0, trfrag(L, boff + 32, 1024), e);
    d = mfma16(a1, trfrag(L, boff + 32 * 256, 1024), d);
    e = mfma16(a1, trfrag(L, boff + 32 + 32 * 256, 1024), e);
    auto s0 = __builtin_amdgcn_permlane16_swap(__float_as_uint(d[0]), __float_as_uint(e[0]), false, false);
    auto s1 = __builtin_amdgcn_permlane16_swap(__float_as_uint(d[1]), __float_as_uint(e[1]), false, false);
    if (lane < 32) {
      const u32 vo = WS_QP + (h * 8 + j) * 2048 + (32 * wv + lane) * 8;
      const v2u g0 = {s0[0], tag}, g1 = {s1[0], tag};
      asm volatile("global_store_dwordx2 %0, %1, %3 sc1\n\tglobal_store_dwordx2 %0, %2, %3 offset:1024 sc1" :: "v"(vo), "v"(g0), "v"(g1), "s"(ws) : "memory");
    }
  }
  if (wv >= 2) {
    if (ffn) {
      ct_wait(L, C_QP, 1);
      const u32 vo = (128 * kq + 64 * (wv - 2) + (lane >> 2)) * 2752 + kap * 64 + (lane & 3) * 16;
      const u32 ld = ldsb + L_W1 + (wv - 2) * 4096;
      const u32 vo1 = vo + 16 * 2752, vo2 = vo + 32 * 2752, vo3 = vo + 48 * 2752;
      DMA16(vo, w1, ld, W_NT);
      DMA16(vo1, w1, ld + 1024, W_NT);
      DMA16(vo2, w1, ld + 2048, W_NT);
      DMA16(vo3, w1, ld + 3072, W_NT);
      VMWAIT(0);
      ct_add(L, C_W1, lane);
      ct_wait(L, C_W1, 2);
      const u32 c = wv - 2;
      const u32 aoff = L_XN + (lane & 1) * 1024 + (128 * kq + 8 * g) * 2;
      const u32 boff = L_W1 + (8 * g + trq) * 64 + c * 32 + trp * 8;
      v4f d = {0.f, 0.f, 0.f, 0.f};
#pragma unroll
      for (int ks = 0; ks < 4; ks++) d = mfma16(lds128(L, aoff + ks * 64), trfrag(L, boff + ks * 32 * 64, 256), d);
      if (lane < 16) {
        const u32 vo_ = WS_HP + (kap * 4 + kq) * 512 + (16 * c + lane) * 8;
        const v2u g0 = {__float_as_uint(d[0]), tag}, g1 = {__float_as_uint(d[1]), tag};
        asm volatile("global_store_dwordx2 %0, %1, %3 sc1\n\tglobal_store_dwordx2 %0, %2, %3 offset:256 sc1" :: "v"(vo_), "v"(g0), "v"(g1), "s"(ws) : "memory");
      }
    }
  } else if (wv == 0) {
    const u32 voQ = WS_QP + h * 8 * 2048 + b * 1024 + lane * 16 + 4096, voQ2 = voQ + 8192;
    const u32 one = 1u, zero = ldsb;
    __builtin_amdgcn_s_sleep(DLYQ);
    v4u p0, p1, p2, p3, p4, p5, p6, p7;
    asm volatile(
        "global_load_dwordx4 %0, %8, %10 offset:-4096 sc1\n\t"
        "global_load_dwordx4 %1, %8, %10 offset:-2048 sc1\n\t"
        "global_load_dwordx4 %2, %8, %10 sc1\n\t"
        "global_load_dwordx4 %3, %8, %10 offset:2048 sc1\n\t"
        "global_load_dwordx4 %4, %9, %10 offset:-4096 sc1\n\t"
        "global_load_dwordx4 %5, %9, %10 offset:-2048 sc1\n\t"
        "global_load_dwordx4 %6, %9, %10 sc1\n\t"
        "global_load_dwordx4 %7, %9, %10 offset:2048 sc1\n\t"
        "ds_write_b32 %12, %11 offset:%13\n\t"
        "s_waitcnt vmcnt(0) lgkmcnt(0)"
        : "=&v"(p0), "=&v"(p1), "=&v"(p2), "=&v"(p3), "=&v"(p4), "=&v"(p5), "=&v"(p6), "=&v"(p7)
        : "v"(voQ), "v"(voQ2), "s"(ws), "v"(one), "v"(zero), "i"(L_CT + C_QP * 4) : "memory");
    for (u32 spin = 0;; spin++) {
      const u32 mn = umin3(umin3(umin3(p0.y, p0.w, p1.y), umin3(p1.w, p2.y, p2.w), umin3(p3.y, p3.w, p4.y)), umin3(umin3(p4.w, p5.y, p5.w), umin3(p6.y, p6.w, p7.y), p7.w), tag);
      const u32 mx = umax3(umax3(umax3(p0.y, p0.w, p1.y), umax3(p1.w, p2.y, p2.w), umax3(p3.y, p3.w, p4.y)), umax3(umax3(p4.w, p5.y, p5.w), umax3(p6.y, p6.w, p7.y), p7.w), tag);
      if (__all(mn == mx) || spin > SPIN_CAP) break;
      asm volatile(
          "global_load_dwordx4 %0, %8, %10 offset:-4096 sc1\n\t"
          "global_load_dwordx4 %1, %8, %10 offset:-2048 sc1\n\t"
          "global_load_dwordx4 %2, %8, %10 sc1\n\t"
          "global_load_dwordx4 %3, %8, %10 offset:2048 sc1\n\t"
          "global_load_dwordx4 %4, %9, %10 offset:-4096 sc1\n\t"
          "global_load_dwordx4 %5, %9, %10 offset:-2048 sc1\n\t"
          "global_load_dwordx4 %6, %9, %10 sc1\n\t"
          "global_load_dwordx4 %7, %9, %10 offset:2048 sc1\n\t"
          "s_waitcnt vmcnt(0)"
          : "=&v"(p0), "=&v"(p1), "=&v"(p2), "=&v"(p3), "=&v"(p4), "=&v"(p5), "=&v"(p6), "=&v"(p7)
          : "v"(voQ), "v"(voQ2), "s"(ws) : "memory");
    }
    const float q0 = ((__uint_as_float(p0.x) + __uint_as_float(p1.x)) + (__uint_as_float(p2.x) + __uint_as_float(p3.x))) +
                     ((__uint_as_float(p4.x) + __uint_as_float(p5.x)) + (__uint_as_float(p6.x) + __uint_as_float(p7.x)));
    const float q1 = ((__uint_as_float(p0.z) + __uint_as_float(p1.z)) + (__uint_as_float(p2.z) + __uint_as_float(p3.z))) +
                     ((__uint_as_float(p4.z) + __uint_as_float(p5.z)) + (__uint_as_float(p6.z) + __uint_as_float(p7.z)));
    *(LAS u32*)(L + L_QB + lane * 4) = pk2(q0, q1);
    ct_add(L, C_Q, lane);
  }
  v2u P0, P1, P2, P3, P4, P5, P6, P7;
  v4u PM;
  float F[8];
  const u32 voP = WS_OP + r * 8 * OPS + lane * 8;
  if (wv == 0) {
    ct_wait(L, C_ML, 8);
    const v4f ma = *(const LAS v4f*)(L + L_MX), mb = *(const LAS v4f*)(L + L_MX + 16);
    const v4f sa = *(const LAS v4f*)(L + L_SM), sb = *(const LAS v4f*)(L + L_SM + 16);
    const float M = fmx(fmx(fmx(ma[0], ma[1]), fmx(ma[2], ma[3])), fmx(fmx(mb[0], mb[1]), fmx(mb[2], mb[3])));
    v4f fa, fb;
#pragma unroll
    for (int k = 0; k < 4; k++) { fa[k] = __builtin_amdgcn_exp2f(ma[k] - M); fb[k] = __builtin_amdgcn_exp2f(mb[k] - M); }
    const float ls = ((fa[0] * sa[0] + fa[1] * sa[1]) + (fa[2] * sa[2] + fa[3] * sa[3])) + ((fb[0] * sb[0] + fb[1] * sb[1]) + (fb[2] * sb[2] + fb[3] * sb[3]));
    if (lane == 0) {
      const u32 vm = WS_OP + (h * 8 + j) * OPS + 512;
      const v4u gq = {__float_as_uint(M), tag, __float_as_uint(ls), tag};
      asm volatile("global_store_dwordx4 %0, %1, %2 sc1\n\ts_nop 1" :: "v"(vm), "v"(gq), "s"(ws) : "memory");
    }
    const u32 voo = WS_OP + (h * 8 + j) * OPS + lane * 8;
    asm volatile("" : "+v"(fa), "+v"(fb) : "v"(voo));
    red_spin(L);
    float o0 = 0.f, o1 = 0.f, o2 = 0.f, o3 = 0.f;
#pragma unroll
    for (int k = 0; k < 4; k++) {
      const v2f v = *(const LAS v2f*)(L + L_RED + k * 512 + lane * 8), u = *(const LAS v2f*)(L + L_RED + (4 + k) * 512 + lane * 8);
      o0 = fmaf(fa[k], v.x, o0); o1 = fmaf(fa[k], v.y, o1); o2 = fmaf(fb[k], u.x, o2); o3 = fmaf(fb[k], u.y, o3);
    }
    const v2u gq = {pk2(o0 + o2, o1 + o3), tag};
    asm volatile("global_store_dwordx2 %0, %1, %2 sc1" :: "v"(voo), "v"(gq), "s"(ws) : "memory");
  } else if (wv == 2) {
    const u32 voM = WS_OP + (r * 8 + (lane & 7)) * OPS + 512, voP1 = voP + OPS;
    ct_wait(L, C_ISS, 8);
    asm volatile("" :: "v"(voM), "v"(voP), "v"(voP1));
    red_spin(L);
    asm volatile("global_load_dwordx4 %0, %1, %2 sc1 ; LD PM" : "=&v"(PM) : "v"(voM), "s"(ws) : "memory");
    __builtin_amdgcn_s_sleep(DLYH1);
    asm volatile(
        "global_load_dwordx2 %0, %8, %9 sc1 ; LD P0\n\t"
        "global_load_dwordx2 %1, %8, %9 offset:640 sc1 ; LD P1\n\t"
        "global_load_dwordx2 %2, %8, %9 offset:1280 sc1 ; LD P2\n\t"
        "global_load_dwordx2 %3, %8, %9 offset:1920 sc1 ; LD P3\n\t"
        "global_load_dwordx2 %4, %8, %9 offset:2560 sc1 ; LD P4\n\t"
        "global_load_dwordx2 %5, %8, %9 offset:3200 sc1 ; LD P5\n\t"
        "global_load_dwordx2 %6, %8, %9 offset:3840 sc1 ; LD P6\n\t"
        "global_load_dwordx2 %7, %10, %9 offset:3840 sc1 ; LD P7"
        : "=&v"(P0), "=&v"(P1), "=&v"(P2), "=&v"(P3), "=&v"(P4), "=&v"(P5), "=&v"(P6), "=&v"(P7)
        : "v"(voP), "s"(ws), "v"(voP1) : "memory");
    asm volatile("s_waitcnt vmcnt(8) ; GUARD PM" : "+v"(PM) :: "memory");
    factors(PM, F);
    asm volatile("" : "+v"(F[0]), "+v"(F[1]), "+v"(F[2]), "+v"(F[3]), "+v"(F[4]), "+v"(F[5]), "+v"(F[6]), "+v"(F[7]));
  } else {
    if (wv == 1) {
      const u32 t0_ = *(const CAS u32*)((const char*)vc + ((b * 32 + h) * 4096 + 3072 + sp * 256) * 256);
      const u32 t1_ = *(const CAS u32*)((const char*)wo + r * 128 * 1024 + xcd * 128);
      const u32 t2_ = *(const CAS u32*)((const char*)w2 + kap2 * 32 * 1024 + xcd * 128);
      const u32 t3_ = *(const CAS u32*)((const char*)b1 + kap2 * 64);
      const u32 t4_ = *(const CAS u32*)((const char*)b2 + xcd * 128);
      const u32 t5_ = *(const CAS u32*)((const char*)out + xcd * 128);
      const u32 t6_ = *(const CAS u32*)((const char*)w1 + (ffn ? 128 * kq * 2752 + kap * 64 : 0));
      asm volatile("" :: "s"(t0_), "s"(t1_), "s"(t2_), "s"(t3_), "s"(t4_), "s"(t5_), "s"(t6_));
    }
    const u32 hi = wv >> 1;
    const u32 vo = (r * 128 + 64 * hi + (lane >> 3)) * 1024 + xcd * 128 + (lane & 7) * 16;
    const u32 ld = ldsb + L_WO + hi * 8192;
    const u32 vo1 = vo + 8 * 1024, vo2 = vo + 16 * 1024, vo3 = vo + 24 * 1024, vo4 = vo + 32 * 1024, vo5 = vo + 40 * 1024, vo6 = vo + 48 * 1024, vo7 = vo + 56 * 1024;
    const u32 vw = ((kap2 + hi) * 32 + (lane >> 3)) * 1024 + xcd * 128 + (lane & 7) * 16;
    const u32 lw = ldsb + L_W2 + hi * 4096;
    const u32 vw1 = vw + 8 * 1024, vw2 = vw + 16 * 1024, vw3 = vw + 24 * 1024;
    ct_wait(L, C_ISS, 8);
    const u32 hbb = lane >> 5, hf = lane & 31, kap2b = kap2 + nk - 1;
    const u32 voH = WS_HP + kap2 * 2048 + hbb * 256 + hf * 8, voHb = WS_HP + kap2b * 2048 + hbb * 256 + hf * 8;
    v2u H0, H1, H2, H3, H4, H5, H6, H7;
    u32 gb0, gb1;
    if (wv == 1) {
      const u32 vb = (kap2 * 32 + hf) * 2, vb2 = (kap2b * 32 + hf) * 2;
      asm volatile(
          "global_load_ushort %8, %12, %15 ; LD GB0\n\t"
          "global_load_ushort %9, %13, %15 ; LD GB1\n\t"
          "global_load_dwordx2 %0, %10, %14 sc1 ; LD H0\n\t"
          "global_load_dwordx2 %1, %10, %14 offset:512 sc1 ; LD H1\n\t"
          "global_load_dwordx2 %2, %10, %14 offset:1024 sc1 ; LD H2\n\t"
          "global_load_dwordx2 %3, %10, %14 offset:1536 sc1 ; LD H3\n\t"
          "global_load_dwordx2 %4, %11, %14 sc1 ; LD H4\n\t"
          "global_load_dwordx2 %5, %11, %14 offset:512 sc1 ; LD H5\n\t"
          "global_load_dwordx2 %6, %11, %14 offset:1024 sc1 ; LD H6\n\t"
          "global_load_dwordx2 %7, %11, %14 offset:1536 sc1 ; LD H7"
          : "=&v"(H0), "=&v"(H1), "=&v"(H2), "=&v"(H3), "=&v"(H4), "=&v"(H5), "=&v"(H6), "=&v"(H7), "=&v"(gb0), "=&v"(gb1)
          : "v"(voH), "v"(voHb), "v"(vb), "v"(vb2), "s"(ws), "s"(b1) : "memory");
    }
    const u32 tD = now32();
#define PD(k) do { while ((int)(now32() - (tD + ((k) * DTAU) / 4)) < 0) {} } while (0)
    DMA16(vo, wo, ld, W_NT);
    PD(1); DMA16(vo1, wo, ld + 1024, W_NT);
    PD(2); DMA16(vo2, wo, ld + 2048, W_NT);
    PD(3); DMA16(vo3, wo, ld + 3072, W_NT);
    PD(4); DMA16(vo4, wo, ld + 4096, W_NT);
    PD(5); DMA16(vo5, wo, ld + 5120, W_NT);
    PD(6); DMA16(vo6, wo, ld + 6144, W_NT);
    PD(7); DMA16(vo7, wo, ld + 7168, W_NT);
    if (hi < nk) {
      PD(8); DMA16(vw, w2, lw, W_NT);
      PD(9); DMA16(vw1, w2, lw + 1024, W_NT);
      PD(10); DMA16(vw2, w2, lw + 2048, W_NT);
      PD(11); DMA16(vw3, w2, lw + 3072, W_NT);
    }
#undef PD
    if (wv == 1) {
      asm volatile("s_waitcnt vmcnt(12) ; GUARD H0 H1 H2 H3 H4 H5 H6 H7 GB0 GB1"
                   : "+v"(H0), "+v"(H1), "+v"(H2), "+v"(H3), "+v"(H4), "+v"(H5), "+v"(H6), "+v"(H7), "+v"(gb0), "+v"(gb1));
      v2u q0 = H0, q1 = H1, q2 = H2, q3 = H3, q4 = H4, q5 = H5, q6 = H6, q7 = H7;
      for (u32 spin = 0;; spin++) {
        const u32 mn = umin3(umin3(q0.y, q1.y, q2.y), umin3(q3.y, q4.y, q5.y), umin3(q6.y, q7.y, tag));
        const u32 mx = umax3(umax3(q0.y, q1.y, q2.y), umax3(q3.y, q4.y, q5.y), umax3(q6.y, q7.y, tag));
        if (__all(mn == mx) || spin > SPIN_CAP) break;
        asm volatile(
            "global_load_dwordx2 %0, %8, %10 sc1\n\t"
            "global_load_dwordx2 %1, %8, %10 offset:512 sc1\n\t"
            "global_load_dwordx2 %2, %8, %10 offset:1024 sc1\n\t"
            "global_load_dwordx2 %3, %8, %10 offset:1536 sc1\n\t"
            "global_load_dwordx2 %4, %9, %10 sc1\n\t"
            "global_load_dwordx2 %5, %9, %10 offset:512 sc1\n\t"
            "global_load_dwordx2 %6, %9, %10 offset:1024 sc1\n\t"
            "global_load_dwordx2 %7, %9, %10 offset:1536 sc1\n\t"
            "s_waitcnt vmcnt(0)"
            : "=&v"(q0), "=&v"(q1), "=&v"(q2), "=&v"(q3), "=&v"(q4), "=&v"(q5), "=&v"(q6), "=&v"(q7)
            : "v"(voH), "v"(voHb), "s"(ws) : "memory");
      }
      const float ha = (__uint_as_float(q0.x) + __uint_as_float(q1.x)) + (__uint_as_float(q2.x) + __uint_as_float(q3.x));
      const float hb = (__uint_as_float(q4.x) + __uint_as_float(q5.x)) + (__uint_as_float(q6.x) + __uint_as_float(q7.x));
      float va = rbf(rbf(ha) + bflo(gb0)), vb_ = rbf(rbf(hb) + bflo(gb1));
      va = va > 0.f ? va : 0.f; vb_ = vb_ > 0.f ? vb_ : 0.f;
      *(LAS u16*)(L + L_HB + hbb * 128 + hf * 2) = (u16)pk2(va * va, 0.f);
      *(LAS u16*)(L + L_HB + hbb * 128 + 64 + hf * 2) = (u16)pk2(vb_ * vb_, 0.f);
    }
    VMWAIT(0);
    ct_add(L, C_WO, lane);
  }
  ct_spin(L, C_WO, 2);
  v4u BW[4];
  v4f dg = {0.f, 0.f, 0.f, 0.f};
  {
    const u32 boff = (8 * g + trq) * 128 + wv * 32 + trp * 8;
#pragma unroll
    for (int ks = 0; ks < 4; ks++) BW[ks] = trfrag(L, L_WO + boff + ks * 4096, 512);
    dg = mfma16(lds128(L, L_HB + (lane & 1) * 128 + g * 16), trfrag(L, L_W2 + boff, 512), dg);
    if (nk == 2) dg = mfma16(lds128(L, L_HB + (lane & 1) * 128 + 64 + g * 16), trfrag(L, L_W2 + boff + 4096, 512), dg);
    asm volatile("" : "+v"(BW[0]), "+v"(BW[1]), "+v"(BW[2]), "+v"(BW[3]), "+v"(dg));
  }
  const u32 voS = WS_SP + ((xcd * 32 + r) * 128 + g * 64 + 16 * wv + i16) * 16;
  const u32 aoffO = L_ON + (lane & 1) * 256 + g * 16;
  asm volatile("" :: "v"(voS), "v"(aoffO));
  if (wv == 2) {
    const u32 voM = WS_OP + (r * 8 + (lane & 7)) * OPS + 512, voP1 = voP + OPS;
    asm volatile("s_waitcnt vmcnt(0) ; GUARD P0 P1 P2 P3 P4 P5 P6 P7"
                 : "+v"(P0), "+v"(P1), "+v"(P2), "+v"(P3), "+v"(P4), "+v"(P5), "+v"(P6), "+v"(P7) :: "memory");
    v2u p0 = P0, p1 = P1, p2 = P2, p3 = P3, p4 = P4, p5 = P5, p6 = P6, p7 = P7;
    {
      const u32 mn = umin3(umin3(umin3(p0.y, p1.y, p2.y), umin3(p3.y, p4.y, p5.y), umin3(p6.y, p7.y, tag)), PM.y, PM.w);
      const u32 mx = umax3(umax3(umax3(p0.y, p1.y, p2.y), umax3(p3.y, p4.y, p5.y), umax3(p6.y, p7.y, tag)), PM.y, PM.w);
      if (!__all(mn == mx)) {
        v4u pm;
        for (u32 spin = 0;; spin++) {
          asm volatile(
              "global_load_dwordx4 %8, %12, %10 sc1\n\t"
              "global_load_dwordx2 %0, %9, %10 sc1\n\t"
              "global_load_dwordx2 %1, %9, %10 offset:640 sc1\n\t"
              "global_load_dwordx2 %2, %9, %10 offset:1280 sc1\n\t"
              "global_load_dwordx2 %3, %9, %10 offset:1920 sc1\n\t"
              "global_load_dwordx2 %4, %9, %10 offset:2560 sc1\n\t"
              "global_load_dwordx2 %5, %9, %10 offset:3200 sc1\n\t"
              "global_load_dwordx2 %6, %9, %10 offset:3840 sc1\n\t"
              "global_load_dwordx2 %7, %11, %10 offset:3840 sc1\n\t"
              "s_waitcnt vmcnt(0)"
              : "=&v"(p0), "=&v"(p1), "=&v"(p2), "=&v"(p3), "=&v"(p4), "=&v"(p5), "=&v"(p6), "=&v"(p7), "=&v"(pm)
              : "v"(voP), "s"(ws), "v"(voP1), "v"(voM) : "memory");
          const u32 mn2 = umin3(umin3(umin3(p0.y, p1.y, p2.y), umin3(p3.y, p4.y, p5.y), umin3(p6.y, p7.y, tag)), pm.y, pm.w);
          const u32 mx2 = umax3(umax3(umax3(p0.y, p1.y, p2.y), umax3(p3.y, p4.y, p5.y), umax3(p6.y, p7.y, tag)), pm.y, pm.w);
          if (__all(mn2 == mx2) || spin > SPIN_CAP) break;
        }
        factors(pm, F);
      }
    }
    const float a0 = fmaf(F[0], bflo(p0.x), F[1] * bflo(p1.x)) + fmaf(F[2], bflo(p2.x), F[3] * bflo(p3.x));
    const float a1 = fmaf(F[0], bfhi(p0.x), F[1] * bfhi(p1.x)) + fmaf(F[2], bfhi(p2.x), F[3] * bfhi(p3.x));
    const float c0 = fmaf(F[4], bflo(p4.x), F[5] * bflo(p5.x)) + fmaf(F[6], bflo(p6.x), F[7] * bflo(p7.x));
    const float c1 = fmaf(F[4], bfhi(p4.x), F[5] * bfhi(p5.x)) + fmaf(F[6], bfhi(p6.x), F[7] * bfhi(p7.x));
    *(LAS u32*)(L + L_ON + lane * 4) = pk2(a0, a1);
    *(LAS u32*)(L + L_ON + 256 + lane * 4) = pk2(c0, c1);
  }
  asm volatile("s_waitcnt lgkmcnt(0)\n\ts_barrier" ::: "memory");
  {
    const v4u a0 = lds128(L, aoffO), a1 = lds128(L, aoffO + 64), a2 = lds128(L, aoffO + 128), a3 = lds128(L, aoffO + 192);
    v4f d = {0.f, 0.f, 0.f, 0.f};
    d = mfma16(a0, BW[0], d); d = mfma16(a1, BW[1], d); d = mfma16(a2, BW[2], d); d = mfma16(a3, BW[3], d);
    auto ss = __builtin_amdgcn_permlane16_swap(__float_as_uint(d[0]), __float_as_uint(d[1]), false, false);
    auto gg = __builtin_amdgcn_permlane16_swap(__float_as_uint(dg[0]), __float_as_uint(dg[1]), false, false);
    if (lane < 32) {
      const v4u gq = {ss[0], tag, gg[0], tag};
      asm volatile("global_store_dwordx4 %0, %1, %2 sc1\n\ts_nop 1" :: "v"(voS), "v"(gq), "s"(ws) : "memory");
    }
  }
}

extern "C" void kernel_launch(void* const* d_in, const int* in_sizes, int n_in, void* d_out, int out_size, void* d_ws, size_t ws_size,
                              hipStream_t stream) {
  fused_decode<<<256, 768, 0, stream>>>((const u16*)d_in[0], (const u16*)d_in[1], (const u16*)d_in[2], (const u16*)d_in[3], (const u16*)d_in[4],
                                        (const u16*)d_in[6], (const u16*)d_in[7], (const u16*)d_in[8], (const u16*)d_in[9], (u16*)d_out,
                                        (unsigned char*)d_ws);
}
```
